```python
import torch

def _softmax(x, axis=-1):
    return torch.softmax(x, dim=axis)
def _gelu(x):
    return torch.nn.functional.gelu(x, approximate="tanh")
def _silu(x):
    return torch.nn.functional.silu(x)
def _sigmoid(x):
    return torch.sigmoid(x)
def _layernorm(x, axis=-1, eps=1e-5):
    m = x.mean(dim=axis, keepdim=True)
    v = x.var(dim=axis, keepdim=True, unbiased=False)
    return (x - m) / torch.sqrt(v + eps)
def _scatter_add(tgt, idx, src, axis=0):
    return tgt.clone().index_add_(axis, idx, src)
def _segment_sum(src, seg, n):
    out = torch.zeros((n,) + src.shape[1:], dtype=src.dtype)
    return out.index_add_(0, seg, src)
def _pad(x, pw):
    flat = tuple(v for pair in reversed(pw) for v in pair)
    return torch.nn.functional.pad(x, flat)

kernel_name = 'interleave_layers__gqa_block_cached__mha_block_cached__decode'

def setup_inputs():

    _w = (lambda a: (lambda t: t.to(torch.bfloat16) if t.is_floating_point() else t)(torch.as_tensor(a)))
    def _A_setup():
        B, Sq, D = 8, 1, 512
        Hq, Hkv, Sk, Dh = 32, 8, 4096, 128
        assert Hq % Hkv == 0, (Hq, Hkv)
        return {
            "x": _w((lambda *s: torch.randn(*s, dtype=torch.bfloat16) * 0.1)(B, Sq, D)),
            "k_cache": _w((lambda *s: torch.randn(*s, dtype=torch.bfloat16) * 0.8)(B, Hkv, Sk, Dh)),
            "v_cache": _w((lambda *s: torch.randn(*s, dtype=torch.bfloat16) * 6.4)(B, Hkv, Sk, Dh)),
            "wq": _w((lambda *s: torch.randn(*s, dtype=torch.bfloat16) * 0.8)(D, Hq * Dh) * (D ** -0.5)),
            "wo": _w((lambda *s: torch.randn(*s, dtype=torch.bfloat16) * 6.4)(Hq * Dh, D) * ((Hq * Dh) ** -0.5)),
        }
    _A_in = _A_setup()

    def _B_setup():
        B, Sq, D = 8, 1, 512
        H, Sk, Dh = 32, 4096, 128
        return {
            "x": _w((lambda *s: torch.randn(*s, dtype=torch.bfloat16) * 0.1)(B, Sq, D)),
            "k_cache": _w((lambda *s: torch.randn(*s, dtype=torch.bfloat16) * 1.6)(B, H, Sk, Dh)),
            "v_cache": _w((lambda *s: torch.randn(*s, dtype=torch.bfloat16) * 3.2)(B, H, Sk, Dh)),
            "wq": _w((lambda *s: torch.randn(*s, dtype=torch.bfloat16) * 1.6)(D, H * Dh) * (D ** -0.5)),
            "wo": _w((lambda *s: torch.randn(*s, dtype=torch.bfloat16) * 3.2)(H * Dh, D) * ((H * Dh) ** -0.5)),
        }
    _B_in = _B_setup()

    return {
        "A_x": _A_in["x"],
        "A_k_cache": _A_in["k_cache"],
        "A_v_cache": _A_in["v_cache"],
        "A_wq": _A_in["wq"],
        "A_wo": _A_in["wo"],
        "B_x": _B_in["x"],
        "B_k_cache": _B_in["k_cache"],
        "B_v_cache": _B_in["v_cache"],
        "B_wq": _B_in["wq"],
        "B_wo": _B_in["wo"],
    }

def reference(A_x, A_k_cache, A_v_cache, A_wq, A_wo, B_x, B_k_cache, B_v_cache, B_wq, B_wo):
    def _A_ref(x, k_cache, v_cache, wq, wo):
        B, Sq, D = x.shape
        Hq, Dh = 32, 128
        Hkv = k_cache.shape[1]
        rep = Hq // Hkv
        q = (lambda x, *d: x.permute(*d))((x @ wq).reshape(B, Sq, Hq, Dh), 0, 2, 1, 3)
        k = (lambda x, n, axis: torch.repeat_interleave(x, n, dim=axis))(k_cache, rep, axis=1)
        v = (lambda x, n, axis: torch.repeat_interleave(x, n, dim=axis))(v_cache, rep, axis=1)
        scores = torch.einsum("bhqd,bhkd->bhqk", q, k) * (Dh ** -0.5)
        p = _softmax(scores, axis=-1)
        o = torch.einsum("bhqk,bhkd->bhqd", p, v)
        return (lambda x, *d: x.permute(*d))(o, 0, 2, 1, 3).reshape(B, Sq, Hq * Dh) @ wo

    def _B_ref(x, k_cache, v_cache, wq, wo):
        B, Sq, D = x.shape
        H, Dh = 32, 128
        q = (lambda x, *d: x.permute(*d))((x @ wq).reshape(B, Sq, H, Dh), 0, 2, 1, 3)
        scores = torch.einsum("bhqd,bhkd->bhqk", q, k_cache) * (Dh ** -0.5)
        p = _softmax(scores, axis=-1)
        o = torch.einsum("bhqk,bhkd->bhqd", p, v_cache)
        return (lambda x, *d: x.permute(*d))(o, 0, 2, 1, 3).reshape(B, Sq, H * Dh) @ wo

    x = A_x
    x = x + _A_ref(_layernorm(x, axis=-1), A_k_cache, A_v_cache, A_wq, A_wo)
    x = x + _B_ref(_layernorm(x, axis=-1), B_k_cache, B_v_cache, B_wq, B_wo)
    x = x + _A_ref(_layernorm(x, axis=-1), A_k_cache, A_v_cache, A_wq, A_wo)
    x = x + _B_ref(_layernorm(x, axis=-1), B_k_cache, B_v_cache, B_wq, B_wo)
    return x
```

```cpp
#include <hip/hip_runtime.h>
#include <hip/hip_bf16.h>
#include <stdint.h>

namespace {
typedef unsigned v4u __attribute__((ext_vector_type(4)));
typedef __bf16 bf16x2 __attribute__((ext_vector_type(2)));
constexpr int NB = 8, DM = 512, NH = 32, SK = 4096, DH = 128, QD = NH * DH;

__device__ __forceinline__ float bf2f(unsigned short u) { return __builtin_bit_cast(float, (unsigned)u << 16); }
__device__ __forceinline__ unsigned short f2bf(float f) { return __builtin_bit_cast(unsigned short, (__bf16)f); }
__device__ __forceinline__ float rbf(float f) { return bf2f(f2bf(f)); }
__device__ __forceinline__ float lo(unsigned u) { return __builtin_bit_cast(float, u << 16); }
__device__ __forceinline__ float hi(unsigned u) { return __builtin_bit_cast(float, u & 0xffff0000u); }
__device__ __forceinline__ float dot2(unsigned a, unsigned b, float c) {
  return __builtin_amdgcn_fdot2_f32_bf16(__builtin_bit_cast(bf16x2, a), __builtin_bit_cast(bf16x2, b), c, false);
}
template <int CTRL> __device__ __forceinline__ float dpp(float x) {
  return __builtin_bit_cast(float, __builtin_amdgcn_mov_dpp(__builtin_bit_cast(int, x), CTRL, 0xf, 0xf, true));
}
__device__ __forceinline__ float row16_sum(float x) {
  x += dpp<0xB1>(x); x += dpp<0x4E>(x); x += dpp<0x141>(x); x += dpp<0x128>(x); return x;
}

__global__ void __launch_bounds__(256) k_qproj(const unsigned short* __restrict__ x, const unsigned short* __restrict__ wq, unsigned short* __restrict__ q) {
  __shared__ float xn[NB][DM];
  __shared__ float red[32][NB][64];
  const int tid = threadIdx.x, lane = tid & 63, w = tid >> 6;
  for (int b = w; b < NB; b += 4) {
    float v[8]; float s = 0.f;
    for (int i = 0; i < 8; ++i) { v[i] = bf2f(x[b * DM + lane * 8 + i]); s += v[i]; }
    for (int o = 32; o; o >>= 1) s += __shfl_xor(s, o);
    const float mean = s * (1.f / DM);
    float ss = 0.f;
    for (int i = 0; i < 8; ++i) { float d = v[i] - mean; ss += d * d; }
    for (int o = 32; o; o >>= 1) ss += __shfl_xor(ss, o);
    const float mb = rbf(mean), vb = rbf(ss * (1.f / DM));
    const float den = rbf(sqrtf(rbf(vb + 1e-5f)));
    for (int i = 0; i < 8; ++i) xn[b][lane * 8 + i] = rbf(rbf(v[i] - mb) / den);
  }
  __syncthreads();
  const int cc = tid & 7, rg = tid >> 3;
  float acc[NB][8];
  for (int b = 0; b < NB; ++b) for (int i = 0; i < 8; ++i) acc[b][i] = 0.f;
  for (int r = 0; r < 16; ++r) {
    const int k = rg * 16 + r;
    const v4u wv = *(const v4u*)(wq + (size_t)k * QD + blockIdx.x * 64 + cc * 8);
    float wf[8] = {lo(wv.x), hi(wv.x), lo(wv.y), hi(wv.y), lo(wv.z), hi(wv.z), lo(wv.w), hi(wv.w)};
    for (int b = 0; b < NB; ++b) { const float xv = xn[b][k]; for (int i = 0; i < 8; ++i) acc[b][i] = fmaf(xv, wf[i], acc[b][i]); }
  }
  for (int b = 0; b < NB; ++b) for (int i = 0; i < 8; ++i) red[rg][b][cc * 8 + i] = acc[b][i];
  __syncthreads();
  for (int o = tid; o < NB * 64; o += 256) {
    const int b = o >> 6, c = o & 63; float s = 0.f;
    for (int g = 0; g < 32; ++g) s += red[g][b][c];
    q[b * QD + blockIdx.x * 64 + c] = f2bf(s);
  }
}

template <int G, int NSPLIT>
__global__ void __launch_bounds__(256) k_attn(const unsigned short* __restrict__ q, const unsigned short* __restrict__ kc, const unsigned short* __restrict__ vc, float* __restrict__ part) {
  constexpr int HKV = NH / G, KPW = SK / NSPLIT / 4, STEPS = KPW / 16;
  __shared__ float mrg[16][G][132];
  const int s = blockIdx.x, kvh = blockIdx.y, b = blockIdx.z;
  const int tid = threadIdx.x, lane = tid & 63, w = tid >> 6, r = lane >> 4, c = lane & 15;
  v4u qp[G];
#pragma unroll
  for (int g = 0; g < G; ++g) qp[g] = *(const v4u*)(q + (size_t)b * QD + (kvh * G + g) * DH + c * 8);
  const size_t base = ((size_t)(b * HKV + kvh) * SK + (size_t)s * (SK / NSPLIT) + (size_t)w * KPW) * DH + lane * 8;
  const v4u* kp = (const v4u*)(kc + base); const v4u* vp = (const v4u*)(vc + base);
  float m[G], l[G], acc[G][8];
#pragma unroll
  for (int g = 0; g < G; ++g) { m[g] = -INFINITY; l[g] = 0.f; for (int i = 0; i < 8; ++i) acc[g][i] = 0.f; }
  v4u kn[4], vn[4];
#pragma unroll
  for (int t = 0; t < 4; ++t) { kn[t] = __builtin_nontemporal_load(kp + t * 64); vn[t] = __builtin_nontemporal_load(vp + t * 64); }
  constexpr float LOG2E = 1.4426950408889634f;
  for (int it = 0; it < STEPS; ++it) {
    v4u kk[4], vv[4];
#pragma unroll
    for (int t = 0; t < 4; ++t) { kk[t] = kn[t]; vv[t] = vn[t]; }
    if (it + 1 < STEPS) {
#pragma unroll
      for (int t = 0; t < 4; ++t) { kn[t] = __builtin_nontemporal_load(kp + ((it + 1) * 4 + t) * 64); vn[t] = __builtin_nontemporal_load(vp + ((it + 1) * 4 + t) * 64); }
    }
#pragma unroll
    for (int g = 0; g < G; ++g) {
      float sc[4];
#pragma unroll
      for (int t = 0; t < 4; ++t) {
        float d = dot2(kk[t].x, qp[g].x, 0.f); d = dot2(kk[t].y, qp[g].y, d); d = dot2(kk[t].z, qp[g].z, d); d = dot2(kk[t].w, qp[g].w, d);
        d = row16_sum(d);
        sc[t] = rbf(rbf(d) * 0.08838834764831845f) * LOG2E;
      }
      const float mn = fmaxf(fmaxf(fmaxf(sc[0], sc[1]), fmaxf(sc[2], sc[3])), m[g]);
      const float al = __builtin_amdgcn_exp2f(m[g] - mn);
      m[g] = mn; l[g] *= al;
#pragma unroll
      for (int i = 0; i < 8; ++i) acc[g][i] *= al;
#pragma unroll
      for (int t = 0; t < 4; ++t) {
        const float p = __builtin_amdgcn_exp2f(sc[t] - mn); l[g] += p;
        const float pb = rbf(p);
        acc[g][0] = fmaf(pb, lo(vv[t].x), acc[g][0]); acc[g][1] = fmaf(pb, hi(vv[t].x), acc[g][1]);
        acc[g][2] = fmaf(pb, lo(vv[t].y), acc[g][2]); acc[g][3] = fmaf(pb, hi(vv[t].y), acc[g][3]);
        acc[g][4] = fmaf(pb, lo(vv[t].z), acc[g][4]); acc[g][5] = fmaf(pb, hi(vv[t].z), acc[g][5]);
        acc[g][6] = fmaf(pb, lo(vv[t].w), acc[g][6]); acc[g][7] = fmaf(pb, hi(vv[t].w), acc[g][7]);
      }
    }
  }
#pragma unroll
  for (int g = 0; g < G; ++g) {
    float* rec = mrg[w * 4 + r][g];
    if (c == 0) { rec[128] = m[g]; rec[129] = l[g]; }
#pragma unroll
    for (int i = 0; i < 8; ++i) rec[c * 8 + i] = acc[g][i];
  }
  __syncthreads();
  for (int o = tid; o < G * DH; o += 256) {
    const int g = o >> 7, d = o & 127;
    float M = -INFINITY;
    for (int j = 0; j < 16; ++j) M = fmaxf(M, mrg[j][g][128]);
    float L = 0.f, O = 0.f;
    for (int j = 0; j < 16; ++j) { const float wt = __builtin_amdgcn_exp2f(mrg[j][g][128] - M); L += wt * mrg[j][g][129]; O += wt * mrg[j][g][d]; }
    float* rec = part + ((size_t)(b * NH + kvh * G + g) * NSPLIT + s) * 132;
    rec[d] = O;
    if (d == 0) { rec[128] = M; rec[129] = L; }
  }
}

template <int NSPLIT>
__global__ void __launch_bounds__(128) k_combine(const float* __restrict__ part, unsigned short* __restrict__ o) {
  const int bh = blockIdx.x, d = threadIdx.x;
  const float* rec = part + (size_t)bh * NSPLIT * 132;
  float M = -INFINITY;
  for (int s = 0; s < NSPLIT; ++s) M = fmaxf(M, rec[s * 132 + 128]);
  float L = 0.f, O = 0.f;
  for (int s = 0; s < NSPLIT; ++s) { const float wt = __builtin_amdgcn_exp2f(rec[s * 132 + 128] - M); L += wt * rec[s * 132 + 129]; O += wt * rec[s * 132 + d]; }
  o[(size_t)bh * DH + d] = f2bf(O / L);
}

__global__ void __launch_bounds__(256) k_oproj(const unsigned short* __restrict__ o, const unsigned short* __restrict__ wo, const unsigned short* __restrict__ xin, unsigned short* __restrict__ xout) {
  __shared__ float red[4][NB * 8];
  const int tid = threadIdx.x, lane = tid & 63, w = tid >> 6;
  float acc[NB][8];
  for (int b = 0; b < NB; ++b) for (int i = 0; i < 8; ++i) acc[b][i] = 0.f;
  for (int r = 0; r < 16; ++r) {
    const int k = r * 256 + tid;
    const v4u wv = *(const v4u*)(wo + (size_t)k * DM + blockIdx.x * 8);
    float wf[8] = {lo(wv.x), hi(wv.x), lo(wv.y), hi(wv.y), lo(wv.z), hi(wv.z), lo(wv.w), hi(wv.w)};
    for (int b = 0; b < NB; ++b) { const float ov = bf2f(o[b * QD + k]); for (int i = 0; i < 8; ++i) acc[b][i] = fmaf(ov, wf[i], acc[b][i]); }
  }
  for (int b = 0; b < NB; ++b) for (int i = 0; i < 8; ++i) {
    float s = acc[b][i];
    for (int of = 32; of; of >>= 1) s += __shfl_xor(s, of);
    if (lane == 0) red[w][b * 8 + i] = s;
  }
  __syncthreads();
  if (tid < NB * 8) {
    const int b = tid >> 3, i = tid & 7, col = blockIdx.x * 8 + i;
    const float y = rbf(red[0][tid] + red[1][tid] + red[2][tid] + red[3][tid]);
    xout[b * DM + col] = f2bf(bf2f(xin[b * DM + col]) + y);
  }
}
}

extern "C" void kernel_launch(void* const* d_in, const int* in_sizes, int n_in, void* d_out, int out_size, void* d_ws, size_t ws_size, hipStream_t stream) {
  typedef const unsigned short* cp;
  cp x0 = (cp)d_in[0], kA = (cp)d_in[1], vA = (cp)d_in[2], wqA = (cp)d_in[3], woA = (cp)d_in[4];
  cp kB = (cp)d_in[6], vB = (cp)d_in[7], wqB = (cp)d_in[8], woB = (cp)d_in[9];
  char* ws = (char*)d_ws;
  unsigned short* q = (unsigned short*)ws;
  unsigned short* o = (unsigned short*)(ws + 65536);
  unsigned short* xa = (unsigned short*)(ws + 131072);
  unsigned short* xb = (unsigned short*)(ws + 139264);
  float* part = (float*)(ws + 262144);
  cp xin = x0;
  for (int layer = 0; layer < 4; ++layer) {
    const bool isA = (layer & 1) == 0;
    unsigned short* xout = layer == 3 ? (unsigned short*)d_out : (layer & 1 ? xb : xa);
    k_qproj<<<64, 256, 0, stream>>>(xin, isA ? wqA : wqB, q);
    if (isA) { k_attn<4, 8><<<dim3(8, 8, NB), 256, 0, stream>>>(q, kA, vA, part); k_combine<8><<<NB * NH, 128, 0, stream>>>(part, o); }
    else { k_attn<1, 2><<<dim3(2, 32, NB), 256, 0, stream>>>(q, kB, vB, part); k_combine<2><<<NB * NH, 128, 0, stream>>>(part, o); }
    k_oproj<<<64, 256, 0, stream>>>(o, isA ? woA : woB, xin, xout);
    xin = xout;
  }
}
```

```cpp
#include <hip/hip_runtime.h>
#include <hip/hip_bf16.h>
#include <stdint.h>

namespace mega {
typedef unsigned short u16;
typedef unsigned v4u __attribute__((ext_vector_type(4)));
typedef unsigned v2u __attribute__((ext_vector_type(2)));
typedef float f32x4 __attribute__((ext_vector_type(4)));
typedef float f32x2 __attribute__((ext_vector_type(2)));
#define LAS __attribute__((address_space(3)))
#define DEV __device__ __forceinline__

constexpr int DM = 512, SK = 4096, QD = 4096;
constexpr float SCALE = 0.08838834764831845f, LOG2E = 1.4426950408889634f;
constexpr unsigned SPIN_CAP = 1u << 17;

constexpr int RECA = 68;
constexpr int RECB = 132;
constexpr int W_P1A = 0, W_P1B = W_P1A + 8 * 16 * 4 * RECA, W_Y = W_P1B + 8 * 16 * RECB, W_Q = W_Y + 2 * 8 * DM,
              W_TOK = W_Q + 2 * QD, W_XF = W_TOK + 128 * 16, W_GROUP = W_XF + 256;
constexpr size_t WS_BYTES = (size_t)2 * W_GROUP * 4;

constexpr int CREC = 132;
constexpr int NSAVE = 5;
constexpr int L_AK = 0, L_AV = 65536, L_RB = 131072, L_U = L_RB + 4 * NSAVE * 1024, L_O = L_U + 4 * 4 * CREC * 4,
              L_Q = L_O + 1024, L_XP = L_Q + 1024, L_ST = L_XP + 1024, L_END = L_ST + 64;
constexpr int L_RED = L_U;
static_assert(L_END <= 163840, "LDS");

#define STAMP(id) do {} while (0)
struct Params { const u16 *x, *kA, *vA, *wqA, *woA, *kB, *vB, *wqB, *woB; u16* out; unsigned* ws; };

#define ACLOB \
  "a0", "a1", "a2", "a3", "a4", "a5", "a6", "a7", "a8", "a9", "a10", "a11", "a12", "a13", "a14", "a15", \
  "a16", "a17", "a18", "a19", "a20", "a21", "a22", "a23", "a24", "a25", "a26", "a27", "a28", "a29", "a30", "a31", \
  "a32", "a33", "a34", "a35", "a36", "a37", "a38", "a39", "a40", "a41", "a42", "a43", "a44", "a45", "a46", "a47", \
  "a48", "a49", "a50", "a51", "a52", "a53", "a54", "a55", "a56", "a57", "a58", "a59", "a60", "a61", "a62", "a63"
#define V8(a, b, c, d, e, f, g, h) "v" #a, "v" #b, "v" #c, "v" #d, "v" #e, "v" #f, "v" #g, "v" #h
#define VCLOB_32_63 V8(32, 33, 34, 35, 36, 37, 38, 39), V8(40, 41, 42, 43, 44, 45, 46, 47), V8(48, 49, 50, 51, 52, 53, 54, 55), V8(56, 57, 58, 59, 60, 61, 62, 63)
#define VCLOB_8_31 V8(8, 9, 10, 11, 12, 13, 14, 15), V8(16, 17, 18, 19, 20, 21, 22, 23), V8(24, 25, 26, 27, 28, 29, 30, 31)
#define VCLOB_16_31 V8(16, 17, 18, 19, 20, 21, 22, 23), V8(24, 25, 26, 27, 28, 29, 30, 31)
#define SCLOB "s84", "s85", "s86", "s87", "s88", "s89", "s90", "s91", "s92", "s93", "s94", "s95"

DEV float bf2f(unsigned u) { return __builtin_bit_cast(float, u << 16); }
DEV unsigned f2bf(float f) { return (unsigned)__builtin_bit_cast(u16, (__bf16)f); }
DEV float rbf(float f) { return bf2f(f2bf(f)); }
DEV float lo(unsigned u) { return __builtin_bit_cast(float, u << 16); }
DEV float hi(unsigned u) { return __builtin_bit_cast(float, u & 0xffff0000u); }
DEV float asf(unsigned u) { return __builtin_bit_cast(float, u); }
DEV unsigned asu(float f) { return __builtin_bit_cast(unsigned, f); }
DEV unsigned tagw(float f, unsigned tag) { return (asu(f) & ~3u) | tag; }
DEV unsigned tagp(float a, float b, unsigned ptag) { return ((f2bf(a) | (f2bf(b) << 16)) & ~0x10001u) | ptag; }
DEV bool none(bool bad) { return __builtin_amdgcn_ballot_w64(bad) == 0ull; }
DEV float ex2(float x) { return __builtin_amdgcn_exp2f(x); }
template <int CTRL> DEV float dppf(float x) { return asf(__builtin_amdgcn_update_dpp(0u, asu(x), CTRL, 0xf, 0xf, true)); }
DEV float rdl(float x, int l) { return asf(__builtin_amdgcn_readlane(asu(x), l)); }
DEV float row_max(float x) {
  x = fmaxf(x, dppf<0xB1>(x)); x = fmaxf(x, dppf<0x4E>(x)); x = fmaxf(x, dppf<0x141>(x)); x = fmaxf(x, dppf<0x140>(x)); return x;
}
DEV float row_sum(float x) {
  x += dppf<0xB1>(x); x += dppf<0x4E>(x); x += dppf<0x141>(x); x += dppf<0x140>(x); return x;
}
DEV float wave_max(float x) { x = row_max(x); return fmaxf(fmaxf(rdl(x, 0), rdl(x, 16)), fmaxf(rdl(x, 32), rdl(x, 48))); }
DEV float wave_sum(float x) { x = row_sum(x); return (rdl(x, 0) + rdl(x, 16)) + (rdl(x, 32) + rdl(x, 48)); }
DEV void lds_barrier() { asm volatile("s_waitcnt lgkmcnt(0)\n\ts_barrier" ::: "memory", ACLOB); }
DEV void cfence() { asm volatile("" ::: "memory", ACLOB); }
DEV int lane_id() {
  int l;
  asm volatile("v_mbcnt_lo_u32_b32 %0, -1, 0\n\tv_mbcnt_hi_u32_b32 %0, -1, %0" : "=v"(l)::ACLOB);
  return l;
}
DEV int fresh_s(int x) { asm volatile("" : "+s"(x)); return x; }
template <int N> DEV void wait_vm() { asm volatile("s_waitcnt vmcnt(%c0)" ::"i"(N) : "memory"); }
DEV v4u make_srd(const void* p, unsigned bytes) {
  v4u s; s.x = (unsigned)(size_t)p; s.y = (unsigned)((size_t)p >> 32) & 0xffffu; s.z = bytes; s.w = 0x00020000u; return s;
}
typedef __amdgpu_buffer_rsrc_t rsrc_t;
DEV unsigned ld1(rsrc_t r, int off, int soff = 0) { return __builtin_amdgcn_raw_buffer_load_b32(r, off, soff, 16); }
DEV v2u ld2(rsrc_t r, int off, int soff = 0) { return __builtin_amdgcn_raw_buffer_load_b64(r, off, soff, 16); }
DEV void st1(rsrc_t r, int off, unsigned v) { __builtin_amdgcn_raw_buffer_store_b32(v, r, off, 0, 16); }
DEV void st2(rsrc_t r, int off, v2u v) { __builtin_amdgcn_raw_buffer_store_b64(v, r, off, 0, 16); }
DEV void st4(rsrc_t r, int off, v4u v) { __builtin_amdgcn_raw_buffer_store_b128(v, r, off, 0, 16); }

template <int F, int IMM> DEV void ld_frag(int voff, v4u srd, int soff) {
  if constexpr (F < 64)
    asm volatile("buffer_load_dwordx4 a[%c0:%c0+3], %1, %2, %3 offen offset:%c4 nt" ::"i"(4 * F), "v"(voff), "s"(srd), "s"(soff), "i"(IMM) : "memory");
  else
    asm volatile("buffer_load_dwordx4 v[%c0:%c0+3], %1, %2, %3 offen offset:%c4 nt" ::"i"(64 + 4 * (F - 64)), "v"(voff), "s"(srd), "s"(soff), "i"(IMM) : "memory");
}
template <int U, int I, int HI> DEV void ld_unit_from(int lk, int lv, v4u sk, v4u sv, int s0, int s1) {
  if constexpr (I < HI) {
    if constexpr (I < 8) ld_frag<16 * U + I, ((I >> 1) & 1) * 2048 + (I & 1) * 128>(lk, sk, (I >> 2) ? s1 : s0);
    else ld_frag<16 * U + I, (I - 8) * 256>(lv, sv, s0);
    ld_unit_from<U, I + 1, HI>(lk, lv, sk, sv, s0, s1);
  }
}
template <int U, int LO, int HI> DEV void ld_unit(int lk, int lv, v4u sk, v4u sv, int logical) {
  const int s0 = logical * 8192, s1 = s0 + 4096;
  asm volatile("s_nop 4" ::: "memory");
  ld_unit_from<U, LO, HI>(lk, lv, sk, sv, s0, s1);
}
DEV void half_burst(int h, int lk, int lv, v4u sk, v4u sv) {
  switch (h) {
    case 0: ld_unit<6, 0, 8>(lk, lv, sk, sv, 0); break;
    case 1: ld_unit<6, 8, 16>(lk, lv, sk, sv, 0); break;
    case 2: ld_unit<0, 0, 8>(lk, lv, sk, sv, 1); break;
    case 3: ld_unit<0, 8, 16>(lk, lv, sk, sv, 1); break;
    case 4: ld_unit<1, 0, 8>(lk, lv, sk, sv, 2); break;
    case 5: ld_unit<1, 8, 16>(lk, lv, sk, sv, 2); break;
    case 6: ld_unit<2, 0, 8>(lk, lv, sk, sv, 3); break;
    case 7: ld_unit<2, 8, 16>(lk, lv, sk, sv, 3); break;
    case 8: ld_unit<3, 0, 8>(lk, lv, sk, sv, 4); break;
    case 9: ld_unit<3, 8, 16>(lk, lv, sk, sv, 4); break;
    case 10: ld_unit<4, 0, 8>(lk, lv, sk, sv, 5); break;
    case 11: ld_unit<4, 8, 16>(lk, lv, sk, sv, 5); break;
    case 12: ld_unit<5, 0, 8>(lk, lv, sk, sv, 6); break;
    case 13: ld_unit<5, 8, 16>(lk, lv, sk, sv, 6); break;
    default: break;
  }
}
constexpr int NHB = 14;

#define MF "v_mfma_f32_16x16x32_bf16 "
#define B_SETUP \
  "s_mov_b32 s84, 0xffff0000\n\ts_mov_b32 s85, 0\n\ts_mov_b32 s86, 0\n\ts_mov_b32 s87, 0x0000ffff\n\t" \
  "s_mov_b32 s88, 0\n\ts_mov_b32 s89, 0xffff0000\n\ts_mov_b32 s90, 0xaaaaaaaa\n\ts_mov_b32 s91, 0xaaaaaaaa\n\ts_nop 1\n\t"
#define B_QK(P, R, BK) \
  MF "v[" #BK ":" #BK "+3], %[qa0], " P "[%c[" R "]:%c[" R "]+3], 0\n\t" \
  MF "v[" #BK "+4:" #BK "+7], %[qa0], " P "[%c[" R "]+8:%c[" R "]+11], 0\n\t" \
  MF "v[" #BK "+8:" #BK "+11], %[qa0], " P "[%c[" R "]+16:%c[" R "]+19], 0\n\t" \
  MF "v[" #BK "+12:" #BK "+15], %[qa0], " P "[%c[" R "]+24:%c[" R "]+27], 0\n\t" \
  MF "v[" #BK ":" #BK "+3], %[qa1], " P "[%c[" R "]+4:%c[" R "]+7], v[" #BK ":" #BK "+3]\n\t" \
  MF "v[" #BK "+4:" #BK "+7], %[qa1], " P "[%c[" R "]+12:%c[" R "]+15], v[" #BK "+4:" #BK "+7]\n\t" \
  MF "v[" #BK "+8:" #BK "+11], %[qa1], " P "[%c[" R "]+20:%c[" R "]+23], v[" #BK "+8:" #BK "+11]\n\t" \
  MF "v[" #BK "+12:" #BK "+15], %[qa1], " P "[%c[" R "]+28:%c[" R "]+31], v[" #BK "+12:" #BK "+15]\n\t"
#define B_MERGE(BK, T) \
  "v_cndmask_b32_e64 v[" #BK "+2], v[" #BK "], v[" #BK "+4], s[84:85]\n\t" \
  "v_cndmask_b32_e64 v[" #BK "+3], v[" #BK "+1], v[" #BK "+5], s[84:85]\n\t" \
  "v_cndmask_b32_e64 v[" #BK "+2], v[" #BK "+2], v[" #BK "+8], s[86:87]\n\t" \
  "v_cndmask_b32_e64 v[" #BK "+3], v[" #BK "+3], v[" #BK "+9], s[86:87]\n\t" \
  "v_cndmask_b32_e64 v[" #BK "+2], v[" #BK "+2], v[" #BK "+12], s[88:89]\n\t" \
  "v_cndmask_b32_e64 v[" #BK "+3], v[" #BK "+3], v[" #BK "+13], s[88:89]\n\t" \
  "v_cndmask_b32_e64 v[" #BK "+2], v[" #BK "+2], v[" #BK "+3], s[90:91]\n\t" \
  "s_nop 1\n\t" \
  "v_add_f32_dpp v[" #BK "+3], v[" #BK "+2], v[" #BK "+2] quad_perm:[1,0,3,2] row_mask:0xf bank_mask:0xf\n\t" \
  "v_cvt_pk_bf16_f32 v[" #BK "+3], v[" #BK "+3], v[" #BK "+3]\n\t" \
  "v_and_b32 v[" #BK "+3], 0xffff0000, v[" #BK "+3]\n\t" \
  "v_mul_f32 v[" #BK "+3], 0x3db504f3, v[" #BK "+3]\n\t" \
  "v_cvt_pk_bf16_f32 v[" #BK "+3], v[" #BK "+3], v[" #BK "+3]\n\t" \
  "v_and_b32 v[" #BK "+3], 0xffff0000, v[" #BK "+3]\n\t" \
  "v_mul_f32 " T ", 0x3fb8aa3b, v[" #BK "+3]\n\t"
#define B_MAX \
  "s_nop 1\n\tv_max_f32_dpp v27, v27, v27 quad_perm:[1,0,3,2] row_mask:0xf bank_mask:0xf\n\t" \
  "s_nop 1\n\tv_max_f32_dpp v27, v27, v27 quad_perm:[2,3,0,1] row_mask:0xf bank_mask:0xf\n\t" \
  "s_nop 1\n\tv_max_f32_dpp v27, v27, v27 row_half_mirror row_mask:0xf bank_mask:0xf\n\t" \
  "s_nop 1\n\tv_max_f32_dpp v27, v27, v27 row_mirror row_mask:0xf bank_mask:0xf\n\t" \
  "s_nop 1\n\t" \
  "v_readlane_b32 s92, v27, 0\n\tv_readlane_b32 s93, v27, 16\n\tv_readlane_b32 s94, v27, 32\n\tv_readlane_b32 s95, v27, 48\n\t" \
  "v_max_f32 v27, s92, %[m]\n\tv_max_f32 v27, s93, v27\n\ts_nop 0\n\tv_max_f32 v27, s94, v27\n\tv_max_f32 v27, s95, v27\n\t" \
  "v_sub_f32 v32, %[m], v27\n\tv_exp_f32 v32, v32\n\tv_mov_b32 %[m], v27\n\t" \
  "v_mul_f32 v28, v28, v32\n\tv_mul_f32 v29, v29, v32\n\tv_mul_f32 v30, v30, v32\n\tv_mul_f32 v31, v31, v32\n\tv_mul_f32 %[l], %[l], v32\n\t"
#define B_EXP(T) "v_sub_f32 " T ", " T ", v27\n\tv_exp_f32 " T ", " T "\n\t"
#define B_ACC(T) "v_add_f32 %[l], %[l], " T "\n\tv_cvt_pk_bf16_f32 " T ", " T ", " T "\n\t"
#define B_AND(S, T, LN) \
  "v_and_b32_dpp v[" #S "], " T ", %[k0] row_newbcast:" #LN " row_mask:0xf bank_mask:0xf\n\t" \
  "v_and_b32_dpp v[" #S "+1], " T ", %[k1] row_newbcast:" #LN " row_mask:0xf bank_mask:0xf\n\t" \
  "v_and_b32_dpp v[" #S "+2], " T ", %[k2] row_newbcast:" #LN " row_mask:0xf bank_mask:0xf\n\t" \
  "v_and_b32_dpp v[" #S "+3], " T ", %[k3] row_newbcast:" #LN " row_mask:0xf bank_mask:0xf\n\t"
#define B_PVM(P, R, O, S) MF "v[28:31], " P "[%c[" R "]+" #O ":%c[" R "]+" #O "+3], v[" #S ":" #S "+3], v[28:31]\n\t"
#define B_PV(P, R, T) \
  B_AND(32, T, 0) B_AND(36, T, 2) B_PVM(P, R, 32, 32) B_AND(40, T, 4) B_PVM(P, R, 36, 36) B_AND(32, T, 6) B_PVM(P, R, 40, 40) \
  B_AND(36, T, 8) B_PVM(P, R, 44, 32) B_AND(40, T, 10) B_PVM(P, R, 48, 36) B_AND(32, T, 12) B_PVM(P, R, 52, 40) \
  B_AND(36, T, 14) B_PVM(P, R, 56, 32) "s_nop 1\n\t" B_PVM(P, R, 60, 36)

struct BState { f32x4 acc; float m, l; };
#define B_OPERANDS(...) \
  : "+{v[28:31]}"(st.acc), [m] "+v"(st.m), [l] "+v"(st.l) \
  : [qa0] "v"(qa0), [qa1] "v"(qa1), [k0] "v"(msk.x), [k1] "v"(msk.y), [k2] "v"(msk.z), [k3] "v"(msk.w), __VA_ARGS__ \
  : "v24", "v25", "v26", "v27", VCLOB_32_63, SCLOB, ACLOB, "memory"
DEV void b_stage_last(BState& st, v4u qa0, v4u qa1, v4u msk) {
  asm volatile(B_SETUP B_QK("v", "r0", 32) "s_nop 7\n\t" B_MERGE(32, "v24") "v_mov_b32 v27, v24\n\t" B_MAX B_EXP("v24") "s_nop 0\n\t" B_ACC("v24") "s_nop 1\n\t"
               B_PV("v", "r0", "v24") "s_nop 7"
               B_OPERANDS([r0] "i"(192)));
}
#define B_STAGE3(P0, P1, P2) \
  B_SETUP B_QK(P0, "r0", 32) B_QK(P1, "r1", 48) B_MERGE(32, "v24") B_QK(P2, "r2", 32) B_MERGE(48, "v25") "s_nop 3\n\t" B_MERGE(32, "v26") \
  "v_max3_f32 v27, v24, v25, v26\n\t" B_MAX B_EXP("v24") B_EXP("v25") B_EXP("v26") "s_nop 0\n\t" B_ACC("v24") B_ACC("v25") B_ACC("v26") "s_nop 1\n\t" \
  B_PV(P0, "r0", "v24") B_PV(P1, "r1", "v25") B_PV(P2, "r2", "v26") "s_nop 7"
DEV void b_stage_012(BState& st, v4u qa0, v4u qa1, v4u msk) {
  asm volatile(B_STAGE3("a", "a", "a") B_OPERANDS([r0] "i"(0), [r1] "i"(64), [r2] "i"(128)));
}
DEV void b_stage_345(BState& st, v4u qa0, v4u qa1, v4u msk) {
  asm volatile(B_STAGE3("a", "v", "v") B_OPERANDS([r0] "i"(192), [r1] "i"(64), [r2] "i"(128)));
}

#define A_RD(BK, XOR, S16) \
  "v_xor_b32 %[t], " #XOR ", %[kb]\n\t" \
  "ds_read_b128 v[" #BK ":" #BK "+3], %[qb] offset:" #S16 "\n\t" \
  "ds_read_b128 v[" #BK "+4:" #BK "+7], %[t]\n\t" \
  "ds_read_b128 v[" #BK "+8:" #BK "+11], %[t] offset:4096\n\t" \
  "ds_read_b128 v[" #BK "+12:" #BK "+15], %[t] offset:8192\n\t" \
  "ds_read_b128 v[" #BK "+16:" #BK "+19], %[t] offset:12288\n\t"
#define A_MM(BK, C0, C1, C2, C3) \
  MF "v[8:11], v[" #BK ":" #BK "+3], v[" #BK "+4:" #BK "+7], " C0 "\n\t" \
  MF "v[12:15], v[" #BK ":" #BK "+3], v[" #BK "+8:" #BK "+11], " C1 "\n\t" \
  MF "v[16:19], v[" #BK ":" #BK "+3], v[" #BK "+12:" #BK "+15], " C2 "\n\t" \
  MF "v[20:23], v[" #BK ":" #BK "+3], v[" #BK "+16:" #BK "+19], " C3 "\n\t"
#define A_SEL(O, H) \
  "v_cndmask_b32_e64 " O ", v[8+" #H "], v[12+" #H "], s[84:85]\n\t" \
  "v_cndmask_b32_e64 " O ", " O ", v[16+" #H "], s[86:87]\n\t" \
  "v_cndmask_b32_e64 " O ", " O ", v[20+" #H "], s[88:89]\n\t"
DEV void a_scores(float& s0, float& s1, float& s2, float& s3, int kb, int qb) {
  int t;
  asm volatile(
      "s_waitcnt lgkmcnt(0)\n\t" B_SETUP
      A_RD(24, 0, 0) A_RD(44, 64, 64) "s_waitcnt lgkmcnt(5)\n\t" A_MM(24, "0", "0", "0", "0")
      A_RD(24, 128, 128) "s_waitcnt lgkmcnt(5)\n\t" A_MM(44, "v[8:11]", "v[12:15]", "v[16:19]", "v[20:23]")
      A_RD(44, 192, 192) "s_waitcnt lgkmcnt(5)\n\t" A_MM(24, "v[8:11]", "v[12:15]", "v[16:19]", "v[20:23]")
      "s_waitcnt lgkmcnt(0)\n\t" A_MM(44, "v[8:11]", "v[12:15]", "v[16:19]", "v[20:23]")
      "s_nop 7\n\t" A_SEL("%[s0]", 0) A_SEL("%[s1]", 1) A_SEL("%[s2]", 2) A_SEL("%[s3]", 3)
      : [s0] "=&v"(s0), [s1] "=&v"(s1), [s2] "=&v"(s2), [s3] "=&v"(s3), [t] "=&v"(t)
      : [kb] "v"(kb), [qb] "v"(qb)
      : VCLOB_8_31, VCLOB_32_63, SCLOB, ACLOB, "memory");
}
#define A_TR(BK, XOR) \
  "v_xor_b32 %[t], " #XOR ", %[vb]\n\t" \
  "ds_read_b64_tr_b16 v[" #BK ":" #BK "+1], %[t]\n\t" \
  "ds_read_b64_tr_b16 v[" #BK "+2:" #BK "+3], %[t] offset:4096\n\t" \
  "ds_read_b64_tr_b16 v[" #BK "+4:" #BK "+5], %[t] offset:8192\n\t" \
  "ds_read_b64_tr_b16 v[" #BK "+6:" #BK "+7], %[t] offset:12288\n\t"
#define A_PV(BK, D) \
  MF "v[" #D ":" #D "+3], v[" #BK ":" #BK "+3], %[p0], 0\n\t" \
  MF "v[" #D ":" #D "+3], v[" #BK "+4:" #BK "+7], %[p1], v[" #D ":" #D "+3]\n\t"
#define A_ST(D, O) "ds_write_b128 %[cb], v[" #D ":" #D "+3] offset:" #O "\n\t"
DEV void a_pv(v4u p0, v4u p1, int vb, int cb) {
  int t;
  asm volatile(
      "s_waitcnt lgkmcnt(0)\n\t"
      A_TR(16, 0) A_TR(24, 32) "s_waitcnt lgkmcnt(4)\n\t" A_PV(16, 32)
      A_TR(16, 64) "s_waitcnt lgkmcnt(4)\n\t" A_PV(24, 36)
      A_TR(24, 96) "s_waitcnt lgkmcnt(4)\n\t" A_PV(16, 40)
      A_TR(16, 128) "s_waitcnt lgkmcnt(4)\n\t" A_PV(24, 44)
      A_TR(24, 160) "s_waitcnt lgkmcnt(4)\n\t" A_PV(16, 48)
      A_TR(16, 192) "s_waitcnt lgkmcnt(4)\n\t" A_PV(24, 52)
      A_TR(24, 224) "s_waitcnt lgkmcnt(4)\n\t" A_PV(16, 56)
      "s_waitcnt lgkmcnt(0)\n\t" A_PV(24, 60)
      "s_mov_b64 s[84:85], exec\n\ts_mov_b32 s86, 0x000f000f\n\ts_mov_b32 s87, 0x000f000f\n\ts_mov_b64 exec, s[86:87]\n\t"
      A_ST(32, 0) A_ST(36, 64) A_ST(40, 128) A_ST(44, 192) A_ST(48, 256) A_ST(52, 320) "s_nop 3\n\t" A_ST(56, 384) A_ST(60, 448)
      "s_mov_b64 exec, s[84:85]"
      : [t] "=&v"(t)
      : [p0] "v"(p0), [p1] "v"(p1), [vb] "v"(vb), [cb] "v"(cb)
      : VCLOB_16_31, VCLOB_32_63, SCLOB, ACLOB, "memory");
}

DEV void dma_a(const Params& p, int b, int team, int j, int w, int lane, int q) {
  const size_t row0 = ((size_t)(b * 8 + team) * SK + 256 * j + 64 * w) * 128;
  const v4u sk = make_srd(p.kA + row0, 16384), sv = make_srd(p.vA + row0, 16384);
  const int r = lane >> 4, c = lane & 15;
  const int pk = r * 256 + ((c ^ r) << 4), pv = r * 256 + ((c ^ (2 * r)) << 4);
  unsigned keep;
  const int mk = __builtin_amdgcn_readfirstlane(L_AK + w * 16384 + q * 4096), mv = mk + (L_AV - L_AK), so = q * 4096;
  asm volatile(
      "s_mov_b32 %0, m0\n\ts_mov_b32 m0, %1\n\ts_nop 4\n\t"
      "buffer_load_dwordx4 %2, %6, %7 offen nt lds\n\t"
      "buffer_load_dwordx4 %3, %6, %7 offen offset:1024 nt lds\n\t"
      "buffer_load_dwordx4 %4, %6, %7 offen offset:2048 nt lds\n\t"
      "buffer_load_dwordx4 %5, %6, %7 offen offset:3072 nt lds\n\t"
      "s_mov_b32 m0, %0"
      : "=&s"(keep) : "s"(mk), "v"(pk), "v"(pk ^ 64), "v"(pk ^ 128), "v"(pk ^ 192), "s"(sk), "s"(so) : "memory");
  asm volatile(
      "s_mov_b32 %0, m0\n\ts_mov_b32 m0, %1\n\ts_nop 4\n\t"
      "buffer_load_dwordx4 %2, %4, %5 offen nt lds\n\t"
      "buffer_load_dwordx4 %3, %4, %5 offen offset:1024 nt lds\n\t"
      "buffer_load_dwordx4 %2, %4, %5 offen offset:2048 nt lds\n\t"
      "buffer_load_dwordx4 %3, %4, %5 offen offset:3072 nt lds\n\t"
      "s_mov_b32 m0, %0"
      : "=&s"(keep) : "s"(mv), "v"(pv), "v"(pv ^ 128), "s"(sv), "s"(so) : "memory");
}

template <int LO = 0, int HI = 8> DEV void load_w(v4u (&wv)[8], const u16* wbase, int ld, int tid) {
  const rsrc_t r = __builtin_amdgcn_make_buffer_rsrc((void*)wbase, 0, 512 * ld * 2, 0x00020000);
  const int off = (8 * (tid >> 2) * ld + 8 * (tid & 3)) * 2;
#pragma unroll
  for (int u = LO; u < HI; ++u) wv[u] = __builtin_amdgcn_raw_buffer_load_b128(r, off, u * ld * 2, 0);
}
DEV float dot2(unsigned a, unsigned b, float c) {
  typedef __bf16 bf16x2 __attribute__((ext_vector_type(2)));
  return __builtin_amdgcn_fdot2_f32_bf16(__builtin_bit_cast(bf16x2, a), __builtin_bit_cast(bf16x2, b), c, false);
}
DEV float gemv32(const v4u (&wv)[8], v4u a, LAS char* L, int tid) {
  float acc[8];
#pragma unroll
  for (int i = 0; i < 8; ++i) acc[i] = 0.f;
#pragma unroll
  for (int u = 0; u < 8; ++u) {
    const unsigned aw = a[u >> 1];
    const unsigned al = (u & 1) ? aw >> 16 : aw & 0xffffu, ah = (u & 1) ? aw & 0xffff0000u : aw << 16;
    acc[0] = dot2(wv[u].x, al, acc[0]); acc[1] = dot2(wv[u].x, ah, acc[1]);
    acc[2] = dot2(wv[u].y, al, acc[2]); acc[3] = dot2(wv[u].y, ah, acc[3]);
    acc[4] = dot2(wv[u].z, al, acc[4]); acc[5] = dot2(wv[u].z, ah, acc[5]);
    acc[6] = dot2(wv[u].w, al, acc[6]); acc[7] = dot2(wv[u].w, ah, acc[7]);
  }
#pragma unroll
  for (int i = 0; i < 8; ++i) acc[i] += dppf<0x124>(acc[i]);
#pragma unroll
  for (int i = 0; i < 8; ++i) acc[i] += dppf<0x128>(acc[i]);
  if ((tid & 15) < 4) {
    LAS f32x4* d = (LAS f32x4*)(L + L_RED + (tid >> 4) * 128 + (tid & 3) * 32);
    d[0] = f32x4{acc[0], acc[1], acc[2], acc[3]}; d[1] = f32x4{acc[4], acc[5], acc[6], acc[7]};
  }
  lds_barrier();
  float s = 0.f;
  if (tid < 64) {
    const LAS float* r = (const LAS float*)(L + L_RED) + (tid >> 5) * 256 + (tid & 31);
    s = ((r[0] + r[32]) + (r[64] + r[96])) + ((r[128] + r[160]) + (r[192] + r[224]));
    const auto sw = __builtin_amdgcn_permlane32_swap(asu(s), asu(s), false, false);
    s = asf(sw[0]) + asf(sw[1]);
  }
  return s;
}
DEV v4u ln_stage(unsigned xn, LAS char* L, int tid) {
  const float a = lo(xn), b = hi(xn);
  const float s1 = wave_sum(a + b), s2 = wave_sum(a * a + b * b);
  *(LAS unsigned*)(L + L_XP + tid * 4) = xn;
  if ((tid & 63) == 0) *(LAS f32x2*)(L + L_ST + (tid >> 6) * 8) = f32x2{s1, s2};
  lds_barrier();
  const f32x4 t0 = *(const LAS f32x4*)(L + L_ST), t1 = *(const LAS f32x4*)(L + L_ST + 16);
  const v4u xv = *(const LAS v4u*)(L + L_XP + (tid >> 2) * 16);
  const float mean = ((t0.x + t0.z) + (t1.x + t1.z)) * (1.f / DM);
  const float var = fmaxf(((t0.y + t0.w) + (t1.y + t1.w)) * (1.f / DM) - mean * mean, 0.f);
  const float mb = rbf(mean), rd = __builtin_amdgcn_rcpf(rbf(__builtin_amdgcn_sqrtf(rbf(rbf(var) + 1e-5f))));
  v4u o;
#pragma unroll
  for (int i = 0; i < 4; ++i) o[i] = f2bf(rbf(lo(xv[i]) - mb) * rd) | (f2bf(rbf(hi(xv[i]) - mb) * rd) << 16);
  return o;
}

__global__ void __launch_bounds__(256, 1) __attribute__((amdgpu_num_vgpr(64))) mega_kernel(Params p) {
  extern __shared__ __attribute__((aligned(16))) char smem[];
  asm volatile("" ::: "v255", "a255");
  LAS char* L = (LAS char*)smem;
  const int w_ = __builtin_amdgcn_readfirstlane(threadIdx.x >> 6), bid_ = blockIdx.x;
  const int grp = (bid_ >> 3) & 1;
  const bool writer = bid_ < 16 && (bid_ & 7) == 0;
  const rsrc_t rs = __builtin_amdgcn_make_buffer_rsrc((void*)(p.ws + (size_t)grp * W_GROUP), 0, W_GROUP * 4, 0x00020000);
#define IDS() \
  const int lane = lane_id(), w = fresh_s(w_), tid = w * 64 + lane, n16 = lane & 15, g4 = lane >> 4, bid = fresh_s(bid_), team = bid & 7, j = bid >> 4; \
  (void)tid; (void)lane; (void)n16; (void)g4; (void)w; (void)team; (void)j
#define STREAM_ARGS() \
  const int lk = (n16 >> 1) * 256 + (n16 & 1) * 64 + g4 * 16, lv = g4 * 2048 + n16 * 16; \
  const size_t srow = ((size_t)((grp + 2 * srow_it) * 32 + 4 * team + (j >> 2)) * SK + 1024 * (j & 3) + 256 * w) * 128; \
  const v4u skB = make_srd(p.kB + srow, 65536), svB = make_srd(p.vB + srow, 65536)
#define BURSTS(N) do { if (may_stream && tok != 0u) { IDS(); STREAM_ARGS(); \
    for (int k_ = 0; k_ < (N) && nb < NHB; ++k_) { half_burst(nb, lk, lv, skB, svB); ++nb; } } } while (0)
#define CATCH_UP(N) do { while (nb < (N)) { half_burst(nb, lk, lv, skB, svB); ++nb; } } while (0)
#define TOKOFF ((W_TOK + (team * 16 + j) * 16 + (srow_it & 3)) * 4)
#define POLL_TOK() do { if (may_stream) tok = __builtin_amdgcn_readfirstlane(tok | ld1(rs, TOKOFF)); } while (0)

  int nb = 0;
  unsigned tok = grp == 0 ? 1u : 0u;
  int stamp_step = 16; (void)stamp_step;
  STAMP(0);

  for (int step = 3; step < 20; ++step) {
    const int it = (step >> 2) - 1, layer = step & 3;
    tok = __builtin_amdgcn_readfirstlane(tok); nb = __builtin_amdgcn_readfirstlane(nb);
    const unsigned tag = 1u + (unsigned)((step >> 1) % 3), ptag = (tag & 1u) | ((tag & 2u) << 15);
    const bool isA = (layer & 1) == 0, lead = step == 3;
    int srow_it = it;
    const bool may_stream = layer == 0 || (layer == 3 && it < 3);
    stamp_step = lead ? 16 : step - 4; if (!lead) STAMP(0);

    if (isA) {
      BURSTS(2);
      float sc[4];
      { IDS(); a_scores(sc[0], sc[1], sc[2], sc[3], L_AK + (64 * w + n16) * 256 + ((g4 ^ n16) << 4), L_Q + (n16 & 3) * 256 + g4 * 16); }
      IDS();
      LAS float* comb = (LAS float*)(L + L_U) + w * 4 * CREC;
      LAS u16* pex = (LAS u16*)comb;
      const int ppos = 32 * (g4 >> 1) + 8 * (n16 >> 2) + 4 * (g4 & 1) + (n16 & 3);
      float mh[4], lh[4], pe[4];
#pragma unroll
      for (int h = 0; h < 4; ++h) { sc[h] = rbf(rbf(sc[h]) * SCALE) * LOG2E; mh[h] = wave_max(sc[h]); }
#pragma unroll
      for (int h = 0; h < 4; ++h) { pe[h] = ex2(sc[h] - mh[h]); pex[h * 64 + ppos] = (u16)f2bf(pe[h]); }
      const v4u pb0 = *(const LAS v4u*)(pex + (n16 & 3) * 64 + 8 * g4), pb1 = *(const LAS v4u*)(pex + (n16 & 3) * 64 + 32 + 8 * g4);
#pragma unroll
      for (int h = 0; h < 4; ++h) lh[h] = wave_sum(pe[h]);
      const float mm = lane == 0 ? mh[0] : lane == 1 ? mh[1] : lane == 2 ? mh[2] : mh[3];
      const float ll = lane == 0 ? lh[0] : lane == 1 ? lh[1] : lane == 2 ? lh[2] : lh[3];
      const int q_ = (lane >> 2) & 3, p_ = lane & 3;
      a_pv(pb0, pb1, L_AV + (64 * w + 4 * g4 + q_) * 256 + ((((p_ >> 1) ^ (2 * (4 * (g4 & 1) + q_)))) << 4) + (p_ & 1) * 8,
           L_U + (w * 4 * CREC + n16 * CREC + 4 * g4) * 4);
      { IDS(); if (lane < 4) *(LAS f32x2*)(L + L_U + ((w * 4 + lane) * CREC + 128) * 4) = f32x2{mm, ll}; }
    } else if (!lead) {
      const bool first = layer == 1;
      const int nrow = grp + 2 * (it + 1);
      const bool pre_a = !first && it < 3;
      v4u qa0 = {0u, 0u, 0u, 0u}, qa1 = {0u, 0u, 0u, 0u}, msk;
      {
        IDS();
        if ((n16 & 2) == 0) {
          qa0 = *(const LAS v4u*)(L + L_Q + (4 * (n16 & 1) + g4) * 16);
          qa1 = *(const LAS v4u*)(L + L_Q + (8 + 4 * (n16 & 1) + g4) * 16);
        }
        const unsigned hm = (n16 & 1) ? 0xffff0000u : 0x0000ffffu;
        const int ri = n16 >> 1;
        msk.x = ri == 0 ? hm : 0u; msk.y = ri == 1 ? hm : 0u; msk.z = ri == 2 ? hm : 0u; msk.w = ri == 3 ? hm : 0u;
        if (first) {
          STREAM_ARGS();
          unsigned spins = 0;
          while (tok == 0u && ++spins < SPIN_CAP) { cfence(); tok = __builtin_amdgcn_readfirstlane(tok | ld1(rs, TOKOFF)); __builtin_amdgcn_s_sleep(2); }
          CATCH_UP(8);
          wait_vm<48>();
          STAMP(7);
        } else if (pre_a) dma_a(p, nrow, team, j, w, lane, 0);
      }
      BState st; st.acc = f32x4{0.f, 0.f, 0.f, 0.f}; st.m = -INFINITY; st.l = 0.f;
      b_stage_last(st, qa0, qa1, msk);
      {
        IDS(); STREAM_ARGS();
        const int sv_off = L_RB + w * (NSAVE * 1024) + lane * 16;
        if (first) {
          asm volatile("ds_write_b128 %0, v[236:239]\n\tds_write_b128 %0, v[240:243] offset:1024\n\tds_write_b128 %0, v[244:247] offset:2048\n\t"
                       "ds_write_b128 %0, v[248:251] offset:3072\n\tds_write_b128 %0, v[252:255] offset:4096" ::"v"(sv_off) : "memory");
          CATCH_UP(12);
          wait_vm<32>();
        } else {
          ld_unit<6, 0, 16 - NSAVE>(lk, lv, skB, svB, 0);
          asm volatile("ds_read_b128 v[236:239], %0\n\tds_read_b128 v[240:243], %0 offset:1024\n\tds_read_b128 v[244:247], %0 offset:2048\n\t"
                       "ds_read_b128 v[248:251], %0 offset:3072\n\tds_read_b128 v[252:255], %0 offset:4096" ::"v"(sv_off) : "memory");
        }
      }
      STAMP(8);
      b_stage_012(st, qa0, qa1, msk);
      {
        IDS();
        if (first) {
          STREAM_ARGS();
          asm volatile("s_waitcnt lgkmcnt(0)" ::: "memory");
          CATCH_UP(14);
          ld_unit<6, 0, 16>(lk, lv, skB, svB, 7);
          if (tid == 0 && it + grp < 4) {
            const rsrc_t rso = __builtin_amdgcn_make_buffer_rsrc((void*)(p.ws + (size_t)(grp ^ 1) * W_GROUP), 0, W_GROUP * 4, 0x00020000);
            st1(rso, (W_TOK + (team * 16 + j) * 16 + it + grp) * 4, 1u);
          }
          wait_vm<16>();
        } else if (pre_a) { dma_a(p, nrow, team, j, w, lane, 1); }
      }
      STAMP(9);
      b_stage_345(st, qa0, qa1, msk);
      if (pre_a) asm volatile("s_waitcnt vmcnt(8) lgkmcnt(0)" ::: "memory");
      else asm volatile("s_waitcnt vmcnt(0) lgkmcnt(0)" ::: "memory");
      if (pre_a) { IDS(); dma_a(p, nrow, team, j, w, lane, 2); }
      b_stage_last(st, qa0, qa1, msk);
      {
        IDS();
        LAS float* comb = (LAS float*)(L + L_U) + w * 4 * CREC;
        const float lsum = wave_sum(st.l) * 0.5f;
        if (n16 < 8) {
          comb[32 * g4 + n16] = st.acc.x; comb[32 * g4 + 8 + n16] = st.acc.y; comb[32 * g4 + 16 + n16] = st.acc.z; comb[32 * g4 + 24 + n16] = st.acc.w;
        }
        if (lane == 0) *(LAS f32x2*)(comb + 128) = f32x2{st.m, lsum};
      }
    }
    if (layer == 3) { srow_it = it + 1; if (!lead) { nb = 0; tok = 0u; } }
    if (!lead) {
      lds_barrier();
      STAMP(1);
      IDS();
      const bool isml = tid >= 128;
      const int h = isml ? tid - 128 : isA ? tid >> 4 : 0;
      if (isml ? h < (isA ? 4 : 1) : tid < (isA ? 64 : 32)) {
        const LAS float* cb = (const LAS float*)(L + L_U) + h * CREC;
        const f32x2 a0 = *(const LAS f32x2*)(cb + 128), a1 = *(const LAS f32x2*)(cb + 4 * CREC + 128), a2 = *(const LAS f32x2*)(cb + 8 * CREC + 128),
                    a3 = *(const LAS f32x2*)(cb + 12 * CREC + 128);
        const float M = fmaxf(fmaxf(a0.x, a1.x), fmaxf(a2.x, a3.x));
        const float f0 = ex2(a0.x - M), f1 = ex2(a1.x - M), f2 = ex2(a2.x - M), f3 = ex2(a3.x - M);
        const int rec = isA ? W_P1A + ((team * 16 + j) * 4 + h) * RECA : W_P1B + (team * 16 + j) * RECB;
        if (isml) {
          st2(rs, (rec + (isA ? 64 : 128)) * 4, v2u{tagw(M, tag), tagw((f0 * a0.y + f1 * a1.y) + (f2 * a2.y + f3 * a3.y), tag)});
        } else if (isA) {
          const int d = 8 * (tid & 15);
          const f32x4 o = (*(const LAS f32x4*)(cb + d) * f0 + *(const LAS f32x4*)(cb + 4 * CREC + d) * f1) +
                          (*(const LAS f32x4*)(cb + 8 * CREC + d) * f2 + *(const LAS f32x4*)(cb + 12 * CREC + d) * f3);
          const f32x4 q = (*(const LAS f32x4*)(cb + d + 4) * f0 + *(const LAS f32x4*)(cb + 4 * CREC + d + 4) * f1) +
                          (*(const LAS f32x4*)(cb + 8 * CREC + d + 4) * f2 + *(const LAS f32x4*)(cb + 12 * CREC + d + 4) * f3);
          st4(rs, (rec + 4 * (tid & 15)) * 4, v4u{tagp(o.x, o.y, ptag), tagp(o.z, o.w, ptag), tagp(q.x, q.y, ptag), tagp(q.z, q.w, ptag)});
        } else {
          const int d = 4 * tid;
          const f32x4 o = (*(const LAS f32x4*)(cb + d) * f0 + *(const LAS f32x4*)(cb + 4 * CREC + d) * f1) +
                          (*(const LAS f32x4*)(cb + 8 * CREC + d) * f2 + *(const LAS f32x4*)(cb + 12 * CREC + d) * f3);
          st4(rs, (rec + d) * 4, v4u{tagw(o.x, tag), tagw(o.y, tag), tagw(o.z, tag), tagw(o.w, tag)});
        }
      }
    }
    if (layer == 3) {
      if (it < 3) {
        IDS();
        v4u wv[8];
        load_w(wv, p.wqA + 512 * team + 32 * j, QD, tid);
        const unsigned xnext = *(const unsigned*)(p.x + (grp + 2 * (it + 1)) * DM + 2 * tid);
        if (lead) { dma_a(p, grp, team, j, w, lane, 0); dma_a(p, grp, team, j, w, lane, 1); dma_a(p, grp, team, j, w, lane, 2); }
        dma_a(p, grp + 2 * (it + 1), team, j, w, lane, 3);
        if (!lead) {
          lds_barrier();
          if (writer) st1(rs, (W_XF + tid) * 4, *(const LAS unsigned*)(L + L_XP + tid * 4));
        }
        const v4u a = ln_stage(xnext, L, tid);
        const float q = gemv32(wv, a, L, tid);
        if (tid < 32) st1(rs, (W_Q + QD + 512 * team + 32 * j + tid) * 4, (f2bf(q) << 16) | tag);
        if (lead) wait_vm<0>();
      }
    }
    STAMP(2);
    if (!lead) {
      v4u wv[8];
      unsigned pr;
      {
        IDS();
        load_w<0, 6>(wv, (isA ? p.woA : p.woB) + (size_t)512 * team * DM + 32 * j, DM, tid);
        unsigned spins = 0;
        float o0 = 0.f, o1 = 0.f;
        if (isA) {
          const int base = (W_P1A + (team * 64 + w) * RECA) * 4;
          v2u ml; unsigned o[16];
          for (;;) {
            cfence();
            ml = ld2(rs, n16 * (4 * RECA * 4) + 256, base);
#pragma unroll
            for (int s = 0; s < 16; ++s) o[s] = ld1(rs, lane * 4, base + s * (4 * RECA * 4));
            POLL_TOK();
            unsigned aa = o[0], oo = o[0];
#pragma unroll
            for (int s = 1; s < 16; ++s) { aa &= o[s]; oo |= o[s]; }
            const unsigned bad = (((aa ^ ptag) | (oo ^ ptag)) & 0x10001u) | (((ml.x ^ tag) | (ml.y ^ tag)) & 3u);
            if (none(bad != 0u) || ++spins > SPIN_CAP) break;
          }
          const float m = asf(ml.x), M = row_max(m), wt = ex2(m - M);
          const float f = wt * __builtin_amdgcn_rcpf(row_sum(wt * asf(ml.y)));
#pragma unroll
          for (int s = 0; s < 16; ++s) { const float fs = rdl(f, s); o0 = fmaf(fs, lo(o[s]), o0); o1 = fmaf(fs, hi(o[s]), o1); }
        } else {
          const int base = (W_P1B + (team * 16 + 4 * w) * RECB) * 4;
          v2u ml, o[4];
          for (;;) {
            cfence();
            ml = ld2(rs, (lane & 3) * (RECB * 4) + 512, base);
#pragma unroll
            for (int s = 0; s < 4; ++s) o[s] = ld2(rs, lane * 8, base + s * (RECB * 4));
            POLL_TOK();
            unsigned aa = ml.x & ml.y, oo = ml.x | ml.y;
#pragma unroll
            for (int s = 0; s < 4; ++s) { aa &= o[s].x & o[s].y; oo |= o[s].x | o[s].y; }
            if (none((((aa ^ tag) | (oo ^ tag)) & 3u) != 0u) || ++spins > SPIN_CAP) break;
          }
          float m = asf(ml.x), M = fmaxf(m, dppf<0xB1>(m)); M = fmaxf(M, dppf<0x4E>(M));
          const float wt = ex2(m - M);
          float ls = wt * asf(ml.y); ls += dppf<0xB1>(ls); ls += dppf<0x4E>(ls);
          const float f = wt * __builtin_amdgcn_rcpf(ls);
#pragma unroll
          for (int s = 0; s < 4; ++s) { const float fs = rdl(f, s); o0 = fmaf(fs, asf(o[s].x), o0); o1 = fmaf(fs, asf(o[s].y), o1); }
        }
        pr = f2bf(o0) | (f2bf(o1) << 16);
      }
      STAMP(3);
      IDS();
      load_w<6, 8>(wv, (isA ? p.woA : p.woB) + (size_t)512 * team * DM + 32 * j, DM, tid);
      *(LAS unsigned*)(L + L_O + tid * 4) = pr;
      lds_barrier();
      const float y = gemv32(wv, *(const LAS v4u*)(L + L_O + (tid >> 2) * 16), L, tid);
      if (tid < 32) st1(rs, (W_Y + ((step & 1) * 8 + team) * DM + 32 * j + tid) * 4, tagw(y, tag));
    }
    BURSTS(1);
    STAMP(4);
    {
      const int npass = layer == 3 ? ((writer && it == 3) ? 1 : 0) : ((writer && layer == 0 && it > 0) ? 2 : 1);
      v4u wv[8];
      { IDS(); load_w(wv, (isA ? p.wqB : p.wqA) + 512 * team + 32 * j, QD, tid); }
      for (int pass = 0; pass < npass; ++pass) {
        IDS();
        const bool fin = layer == 3 || pass == 1;
        const unsigned ytag = pass == 1 ? 1u + (unsigned)(((step - 1) >> 1) % 3) : tag;
        const int ybase = (W_Y + (((step & 1) ^ pass) * 8) * DM) * 4;
        v2u yv[8];
        unsigned spins = 0;
        for (;;) {
          cfence();
#pragma unroll
          for (int k = 0; k < 8; ++k) yv[k] = ld2(rs, tid * 8, ybase + k * (DM * 4));
          POLL_TOK();
          unsigned aa = ~0u, oo = 0u;
#pragma unroll
          for (int k = 0; k < 8; ++k) { aa &= yv[k].x & yv[k].y; oo |= yv[k].x | yv[k].y; }
          if (none((((aa ^ ytag) | (oo ^ ytag)) & 3u) != 0u) || ++spins > SPIN_CAP) break;
        }
        if (pass == 0) STAMP(5);
        const float y0 = ((asf(yv[0].x) + asf(yv[1].x)) + (asf(yv[2].x) + asf(yv[3].x))) + ((asf(yv[4].x) + asf(yv[5].x)) + (asf(yv[6].x) + asf(yv[7].x)));
        const float y1 = ((asf(yv[0].y) + asf(yv[1].y)) + (asf(yv[2].y) + asf(yv[3].y))) + ((asf(yv[4].y) + asf(yv[5].y)) + (asf(yv[6].y) + asf(yv[7].y)));
        const unsigned xo = pass == 1 ? ld1(rs, (W_XF + tid) * 4) : *(const LAS unsigned*)(L + L_XP + tid * 4);
        const unsigned xn = f2bf(lo(xo) + rbf(y0)) | (f2bf(hi(xo) + rbf(y1)) << 16);
        if (fin) {
          *(unsigned*)(p.out + (grp + 2 * (layer == 3 ? it : it - 1)) * DM + 2 * tid) = xn;
        } else {
          const v4u a = ln_stage(xn, L, tid);
          const float q = gemv32(wv, a, L, tid);
          if (tid < 32) st1(rs, (W_Q + (step & 1) * QD + 512 * team + 32 * j + tid) * 4, (f2bf(q) << 16) | tag);
        }
      }
    }
    if (step == 19) break;
    BURSTS(1);
    STAMP(6);
    {
      IDS();
      const int qoff = W_Q + (step & 1) * QD + 512 * team + (isA ? 128 * (j >> 2) : 0);
      if (!isA || w == 0) {
        unsigned spins = 0; v2u qv;
        for (;;) {
          cfence();
          qv = ld2(rs, tid * 8, qoff * 4);
          POLL_TOK();
          if (none((((qv.x ^ tag) | (qv.y ^ tag)) & 3u) != 0u) || ++spins > SPIN_CAP) break;
        }
        *(LAS unsigned*)(L + L_Q + tid * 4) = (qv.x >> 16) | (qv.y & 0xffff0000u);
      }
      lds_barrier();
    }
  }
}
}

extern "C" void kernel_launch(void* const* d_in, const int* in_sizes, int n_in, void* d_out, int out_size, void* d_ws, size_t ws_size, hipStream_t stream) {
  typedef const mega::u16* cp;
  mega::Params p;
  p.x = (cp)d_in[0]; p.kA = (cp)d_in[1]; p.vA = (cp)d_in[2]; p.wqA = (cp)d_in[3]; p.woA = (cp)d_in[4];
  p.kB = (cp)d_in[6]; p.vB = (cp)d_in[7]; p.wqB = (cp)d_in[8]; p.woB = (cp)d_in[9];
  p.out = (mega::u16*)d_out; p.ws = (unsigned*)d_ws;
  (void)hipMemsetAsync(d_ws, 0, mega::WS_BYTES, stream);
  (void)hipFuncSetAttribute((const void*)mega::mega_kernel, hipFuncAttributeMaxDynamicSharedMemorySize, 163840);
  mega::mega_kernel<<<256, 256, 163840, stream>>>(p);
}
```

```cpp
#include <hip/hip_runtime.h>
#include <hip/hip_bf16.h>
#include <stdint.h>

namespace mega {
typedef unsigned short u16;
typedef unsigned v4u __attribute__((ext_vector_type(4)));
typedef unsigned v2u __attribute__((ext_vector_type(2)));
typedef float f32x4 __attribute__((ext_vector_type(4)));
typedef float f32x2 __attribute__((ext_vector_type(2)));
#define LAS __attribute__((address_space(3)))
#define DEV __device__ __forceinline__

constexpr int DM = 512, SK = 4096, QD = 4096;
constexpr float SCALE = 0.08838834764831845f, LOG2E = 1.4426950408889634f;
constexpr unsigned SPIN_CAP = 1u << 17;

constexpr int RECA = 68;
constexpr int RECB = 132;
constexpr int SZ_P1A = 8 * 16 * 4 * RECA, SZ_P1B = 8 * 16 * RECB;
constexpr int W_P1A = 0, W_P1B = W_P1A + 2 * SZ_P1A, W_Y = W_P1B + 2 * SZ_P1B, W_Q = W_Y + 4 * 8 * DM,
              W_TOK = W_Q + 7 * QD, W_GROUP = W_TOK + 128 * 16;
constexpr size_t WS_BYTES = (size_t)2 * W_GROUP * 4;

constexpr int CREC = 132;
constexpr int NSAVE = 5;
constexpr int L_AK = 0, L_AV = 65536, L_RB = 131072, L_U = L_RB + 4 * NSAVE * 1024, L_O = L_U + 4 * 4 * CREC * 4,
              L_Q = L_O + 1024, L_XP = L_Q + 1024, L_ST = L_XP + 1024, L_XS = L_ST + 64, L_END = L_XS + 64;
constexpr int L_RED = L_U;
static_assert(L_END <= 163840, "LDS");

#define STAMP(id) do {} while (0)
#define SUB(id) do {} while (0)
struct Params { const u16 *x, *kA, *vA, *wqA, *woA, *kB, *vB, *wqB, *woB; u16* out; unsigned* ws; };

#define ACLOB \
  "a0", "a1", "a2", "a3", "a4", "a5", "a6", "a7", "a8", "a9", "a10", "a11", "a12", "a13", "a14", "a15", \
  "a16", "a17", "a18", "a19", "a20", "a21", "a22", "a23", "a24", "a25", "a26", "a27", "a28", "a29", "a30", "a31", \
  "a32", "a33", "a34", "a35", "a36", "a37", "a38", "a39", "a40", "a41", "a42", "a43", "a44", "a45", "a46", "a47", \
  "a48", "a49", "a50", "a51", "a52", "a53", "a54", "a55", "a56", "a57", "a58", "a59", "a60", "a61", "a62", "a63"
#define V8(a, b, c, d, e, f, g, h) "v" #a, "v" #b, "v" #c, "v" #d, "v" #e, "v" #f, "v" #g, "v" #h
#define VCLOB_32_63 V8(32, 33, 34, 35, 36, 37, 38, 39), V8(40, 41, 42, 43, 44, 45, 46, 47), V8(48, 49, 50, 51, 52, 53, 54, 55), V8(56, 57, 58, 59, 60, 61, 62, 63)
#define VCLOB_8_31 V8(8, 9, 10, 11, 12, 13, 14, 15), V8(16, 17, 18, 19, 20, 21, 22, 23), V8(24, 25, 26, 27, 28, 29, 30, 31)
#define VCLOB_16_31 V8(16, 17, 18, 19, 20, 21, 22, 23), V8(24, 25, 26, 27, 28, 29, 30, 31)
#define SCLOB "s84", "s85", "s86", "s87", "s88", "s89", "s90", "s91", "s92", "s93", "s94", "s95"

DEV float bf2f(unsigned u) { return __builtin_bit_cast(float, u << 16); }
DEV unsigned f2bf(float f) { return (unsigned)__builtin_bit_cast(u16, (__bf16)f); }
DEV float rbf(float f) { return bf2f(f2bf(f)); }
DEV float lo(unsigned u) { return __builtin_bit_cast(float, u << 16); }
DEV float hi(unsigned u) { return __builtin_bit_cast(float, u & 0xffff0000u); }
DEV float asf(unsigned u) { return __builtin_bit_cast(float, u); }
DEV unsigned asu(float f) { return __builtin_bit_cast(unsigned, f); }
DEV unsigned tagw(float f, unsigned tag) { return (asu(f) & ~3u) | tag; }
DEV unsigned tagp(float a, float b, unsigned ptag) { return ((f2bf(a) | (f2bf(b) << 16)) & ~0x10001u) | ptag; }
DEV bool none(bool bad) { return __builtin_amdgcn_ballot_w64(bad) == 0ull; }
DEV float ex2(float x) { return __builtin_amdgcn_exp2f(x); }
template <int CTRL> DEV float dppf(float x) { return asf(__builtin_amdgcn_update_dpp(0u, asu(x), CTRL, 0xf, 0xf, true)); }
DEV float rdl(float x, int l) { return asf(__builtin_amdgcn_readlane(asu(x), l)); }
DEV float row_max(float x) {
  x = fmaxf(x, dppf<0xB1>(x)); x = fmaxf(x, dppf<0x4E>(x)); x = fmaxf(x, dppf<0x141>(x)); x = fmaxf(x, dppf<0x140>(x)); return x;
}
DEV float row_sum(float x) {
  x += dppf<0xB1>(x); x += dppf<0x4E>(x); x += dppf<0x141>(x); x += dppf<0x140>(x); return x;
}
template <int CTRL, int RM> DEV float dppk(float x) { return asf(__builtin_amdgcn_update_dpp(asu(x), asu(x), CTRL, RM, 0xf, false)); }
DEV float wave_max(float x) {
  x = row_max(x); x = fmaxf(x, dppk<0x142, 0xa>(x)); x = fmaxf(x, dppk<0x143, 0xc>(x)); return rdl(x, 63);
}
DEV float wave_sum(float x) {
  x = row_sum(x);
  x += asf(__builtin_amdgcn_update_dpp(0u, asu(x), 0x142, 0xa, 0xf, false));
  x += asf(__builtin_amdgcn_update_dpp(0u, asu(x), 0x143, 0xc, 0xf, false));
  return rdl(x, 63);
}
DEV void lds_barrier() { asm volatile("s_waitcnt lgkmcnt(0)\n\ts_barrier" ::: "memory", ACLOB); }
DEV void cfence() { asm volatile("" ::: "memory", ACLOB); }
DEV int lane_id() {
  int l;
  asm volatile("v_mbcnt_lo_u32_b32 %0, -1, 0\n\tv_mbcnt_hi_u32_b32 %0, -1, %0" : "=v"(l)::ACLOB);
  return l;
}
DEV int fresh_s(int x) { asm volatile("" : "+s"(x)); return x; }
template <int N> DEV void wait_vm() { asm volatile("s_waitcnt vmcnt(%c0)" ::"i"(N) : "memory"); }
DEV v4u make_srd(const void* p, unsigned bytes) {
  v4u s; s.x = (unsigned)(size_t)p; s.y = (unsigned)((size_t)p >> 32) & 0xffffu; s.z = bytes; s.w = 0x00020000u; return s;
}
typedef __amdgpu_buffer_rsrc_t rsrc_t;
DEV unsigned ld1(rsrc_t r, int off, int soff = 0) { return __builtin_amdgcn_raw_buffer_load_b32(r, off, soff, 16); }
DEV v2u ld2(rsrc_t r, int off, int soff = 0) { return __builtin_amdgcn_raw_buffer_load_b64(r, off, soff, 16); }
DEV void st1(rsrc_t r, int off, unsigned v) { __builtin_amdgcn_raw_buffer_store_b32(v, r, off, 0, 16); }
DEV void st2(rsrc_t r, int off, v2u v) { __builtin_amdgcn_raw_buffer_store_b64(v, r, off, 0, 16); }
DEV void st4(rsrc_t r, int off, v4u v) { __builtin_amdgcn_raw_buffer_store_b128(v, r, off, 0, 16); }

template <int F, int IMM> DEV void ld_frag(int voff, v4u srd, int soff) {
  if constexpr (F < 64)
    asm volatile("buffer_load_dwordx4 a[%c0:%c0+3], %1, %2, %3 offen offset:%c4 nt" ::"i"(4 * F), "v"(voff), "s"(srd), "s"(soff), "i"(IMM) : "memory");
  else
    asm volatile("buffer_load_dwordx4 v[%c0:%c0+3], %1, %2, %3 offen offset:%c4 nt" ::"i"(64 + 4 * (F - 64)), "v"(voff), "s"(srd), "s"(soff), "i"(IMM) : "memory");
}
template <int U, int I, int HI> DEV void ld_unit_from(int lk, int lv, v4u sk, v4u sv, int s0, int s1) {
  if constexpr (I < HI) {
    if constexpr (I < 8) ld_frag<16 * U + I, ((I >> 1) & 1) * 2048 + (I & 1) * 128>(lk, sk, (I >> 2) ? s1 : s0);
    else ld_frag<16 * U + I, (I - 8) * 256>(lv, sv, s0);
    ld_unit_from<U, I + 1, HI>(lk, lv, sk, sv, s0, s1);
  }
}
template <int U, int LO, int HI> DEV void ld_unit(int lk, int lv, v4u sk, v4u sv, int logical) {
  const int s0 = logical * 8192, s1 = s0 + 4096;
  asm volatile("s_nop 4" ::: "memory");
  ld_unit_from<U, LO, HI>(lk, lv, sk, sv, s0, s1);
}
DEV void half_burst(int h, int lk, int lv, v4u sk, v4u sv) {
  switch (h) {
    case 0: ld_unit<6, 0, 8>(lk, lv, sk, sv, 0); break;
    case 1: ld_unit<6, 8, 16>(lk, lv, sk, sv, 0); break;
    case 2: ld_unit<0, 0, 8>(lk, lv, sk, sv, 1); break;
    case 3: ld_unit<0, 8, 16>(lk, lv, sk, sv, 1); break;
    case 4: ld_unit<1, 0, 8>(lk, lv, sk, sv, 2); break;
    case 5: ld_unit<1, 8, 16>(lk, lv, sk, sv, 2); break;
    case 6: ld_unit<2, 0, 8>(lk, lv, sk, sv, 3); break;
    case 7: ld_unit<2, 8, 16>(lk, lv, sk, sv, 3); break;
    case 8: ld_unit<3, 0, 8>(lk, lv, sk, sv, 4); break;
    case 9: ld_unit<3, 8, 16>(lk, lv, sk, sv, 4); break;
    case 10: ld_unit<4, 0, 8>(lk, lv, sk, sv, 5); break;
    case 11: ld_unit<4, 8, 16>(lk, lv, sk, sv, 5); break;
    case 12: ld_unit<5, 0, 8>(lk, lv, sk, sv, 6); break;
    case 13: ld_unit<5, 8, 16>(lk, lv, sk, sv, 6); break;
    default: break;
  }
}
constexpr int NHB = 14;
#define NOTOK 2
#define ND1 0
#define ND2 0
#define NB0 1
#define NB1 1
#define NB2 1
#define NB3 1
#define NB4 1
#define NB5 1
#define TOK_POS 4

#define MF "v_mfma_f32_16x16x32_bf16 "
#define B_SETUP \
  "s_mov_b32 s84, 0xffff0000\n\ts_mov_b32 s85, 0\n\ts_mov_b32 s86, 0\n\ts_mov_b32 s87, 0x0000ffff\n\t" \
  "s_mov_b32 s88, 0\n\ts_mov_b32 s89, 0xffff0000\n\ts_mov_b32 s90, 0xaaaaaaaa\n\ts_mov_b32 s91, 0xaaaaaaaa\n\ts_nop 1\n\t"
#define B_QK(P, R, BK) \
  MF "v[" #BK ":" #BK "+3], %[qa0], " P "[%c[" R "]:%c[" R "]+3], 0\n\t" \
  MF "v[" #BK "+4:" #BK "+7], %[qa0], " P "[%c[" R "]+8:%c[" R "]+11], 0\n\t" \
  MF "v[" #BK "+8:" #BK "+11], %[qa0], " P "[%c[" R "]+16:%c[" R "]+19], 0\n\t" \
  MF "v[" #BK "+12:" #BK "+15], %[qa0], " P "[%c[" R "]+24:%c[" R "]+27], 0\n\t" \
  MF "v[" #BK ":" #BK "+3], %[qa1], " P "[%c[" R "]+4:%c[" R "]+7], v[" #BK ":" #BK "+3]\n\t" \
  MF "v[" #BK "+4:" #BK "+7], %[qa1], " P "[%c[" R "]+12:%c[" R "]+15], v[" #BK "+4:" #BK "+7]\n\t" \
  MF "v[" #BK "+8:" #BK "+11], %[qa1], " P "[%c[" R "]+20:%c[" R "]+23], v[" #BK "+8:" #BK "+11]\n\t" \
  MF "v[" #BK "+12:" #BK "+15], %[qa1], " P "[%c[" R "]+28:%c[" R "]+31], v[" #BK "+12:" #BK "+15]\n\t"
#define B_MERGE(BK, T) \
  "v_cndmask_b32_e64 v[" #BK "+2], v[" #BK "], v[" #BK "+4], s[84:85]\n\t" \
  "v_cndmask_b32_e64 v[" #BK "+3], v[" #BK "+1], v[" #BK "+5], s[84:85]\n\t" \
  "v_cndmask_b32_e64 v[" #BK "+2], v[" #BK "+2], v[" #BK "+8], s[86:87]\n\t" \
  "v_cndmask_b32_e64 v[" #BK "+3], v[" #BK "+3], v[" #BK "+9], s[86:87]\n\t" \
  "v_cndmask_b32_e64 v[" #BK "+2], v[" #BK "+2], v[" #BK "+12], s[88:89]\n\t" \
  "v_cndmask_b32_e64 v[" #BK "+3], v[" #BK "+3], v[" #BK "+13], s[88:89]\n\t" \
  "v_cndmask_b32_e64 v[" #BK "+2], v[" #BK "+2], v[" #BK "+3], s[90:91]\n\t" \
  "s_nop 1\n\t" \
  "v_add_f32_dpp v[" #BK "+3], v[" #BK "+2], v[" #BK "+2] quad_perm:[1,0,3,2] row_mask:0xf bank_mask:0xf\n\t" \
  "v_cvt_pk_bf16_f32 v[" #BK "+3], v[" #BK "+3], v[" #BK "+3]\n\t" \
  "v_and_b32 v[" #BK "+3], 0xffff0000, v[" #BK "+3]\n\t" \
  "v_mul_f32 v[" #BK "+3], 0x3db504f3, v[" #BK "+3]\n\t" \
  "v_cvt_pk_bf16_f32 v[" #BK "+3], v[" #BK "+3], v[" #BK "+3]\n\t" \
  "v_and_b32 v[" #BK "+3], 0xffff0000, v[" #BK "+3]\n\t" \
  "v_mul_f32 " T ", 0x3fb8aa3b, v[" #BK "+3]\n\t"
#define B_MAX \
  "s_nop 1\n\tv_max_f32_dpp v27, v27, v27 quad_perm:[1,0,3,2] row_mask:0xf bank_mask:0xf\n\t" \
  "s_nop 1\n\tv_max_f32_dpp v27, v27, v27 quad_perm:[2,3,0,1] row_mask:0xf bank_mask:0xf\n\t" \
  "s_nop 1\n\tv_max_f32_dpp v27, v27, v27 row_half_mirror row_mask:0xf bank_mask:0xf\n\t" \
  "s_nop 1\n\tv_max_f32_dpp v27, v27, v27 row_mirror row_mask:0xf bank_mask:0xf\n\t" \
  "s_nop 1\n\t" \
  "v_readlane_b32 s92, v27, 0\n\tv_readlane_b32 s93, v27, 16\n\tv_readlane_b32 s94, v27, 32\n\tv_readlane_b32 s95, v27, 48\n\t" \
  "v_max_f32 v27, s92, %[m]\n\tv_max_f32 v27, s93, v27\n\ts_nop 0\n\tv_max_f32 v27, s94, v27\n\tv_max_f32 v27, s95, v27\n\t" \
  "v_sub_f32 v32, %[m], v27\n\tv_exp_f32 v32, v32\n\tv_mov_b32 %[m], v27\n\t" \
  "v_mul_f32 v28, v28, v32\n\tv_mul_f32 v29, v29, v32\n\tv_mul_f32 v30, v30, v32\n\tv_mul_f32 v31, v31, v32\n\tv_mul_f32 %[l], %[l], v32\n\t"
#define B_EXP(T) "v_sub_f32 " T ", " T ", v27\n\tv_exp_f32 " T ", " T "\n\t"
#define B_ACC(T) "v_add_f32 %[l], %[l], " T "\n\tv_cvt_pk_bf16_f32 " T ", " T ", " T "\n\t"
#define B_AND(S, T, LN) \
  "v_and_b32_dpp v[" #S "], " T ", %[k0] row_newbcast:" #LN " row_mask:0xf bank_mask:0xf\n\t" \
  "v_and_b32_dpp v[" #S "+1], " T ", %[k1] row_newbcast:" #LN " row_mask:0xf bank_mask:0xf\n\t" \
  "v_and_b32_dpp v[" #S "+2], " T ", %[k2] row_newbcast:" #LN " row_mask:0xf bank_mask:0xf\n\t" \
  "v_and_b32_dpp v[" #S "+3], " T ", %[k3] row_newbcast:" #LN " row_mask:0xf bank_mask:0xf\n\t"
#define B_PVM(P, R, O, S) MF "v[28:31], " P "[%c[" R "]+" #O ":%c[" R "]+" #O "+3], v[" #S ":" #S "+3], v[28:31]\n\t"
#define B_PV(P, R, T) \
  B_AND(32, T, 0) B_AND(36, T, 2) B_PVM(P, R, 32, 32) B_AND(40, T, 4) B_PVM(P, R, 36, 36) B_AND(32, T, 6) B_PVM(P, R, 40, 40) \
  B_AND(36, T, 8) B_PVM(P, R, 44, 32) B_AND(40, T, 10) B_PVM(P, R, 48, 36) B_AND(32, T, 12) B_PVM(P, R, 52, 40) \
  B_AND(36, T, 14) B_PVM(P, R, 56, 32) "s_nop 1\n\t" B_PVM(P, R, 60, 36)

struct BState { f32x4 acc; float m, l; };
#define B_OPERANDS(...) \
  : "+{v[28:31]}"(st.acc), [m] "+v"(st.m), [l] "+v"(st.l) \
  : [qa0] "v"(qa0), [qa1] "v"(qa1), [k0] "v"(msk.x), [k1] "v"(msk.y), [k2] "v"(msk.z), [k3] "v"(msk.w), __VA_ARGS__ \
  : "v24", "v25", "v26", "v27", VCLOB_32_63, SCLOB, ACLOB, "memory"
DEV void b_stage_last(BState& st, v4u qa0, v4u qa1, v4u msk) {
  asm volatile(B_SETUP B_QK("v", "r0", 32) "s_nop 7\n\t" B_MERGE(32, "v24") "v_mov_b32 v27, v24\n\t" B_MAX B_EXP("v24") "s_nop 0\n\t" B_ACC("v24") "s_nop 1\n\t"
               B_PV("v", "r0", "v24") "s_nop 7"
               B_OPERANDS([r0] "i"(192)));
}
#define B_STAGE3(P0, P1, P2) \
  B_SETUP B_QK(P0, "r0", 32) B_QK(P1, "r1", 48) B_MERGE(32, "v24") B_QK(P2, "r2", 32) B_MERGE(48, "v25") "s_nop 3\n\t" B_MERGE(32, "v26") \
  "v_max3_f32 v27, v24, v25, v26\n\t" B_MAX B_EXP("v24") B_EXP("v25") B_EXP("v26") "s_nop 0\n\t" B_ACC("v24") B_ACC("v25") B_ACC("v26") "s_nop 1\n\t" \
  B_PV(P0, "r0", "v24") B_PV(P1, "r1", "v25") B_PV(P2, "r2", "v26") "s_nop 7"
DEV void b_stage_012(BState& st, v4u qa0, v4u qa1, v4u msk) {
  asm volatile(B_STAGE3("a", "a", "a") B_OPERANDS([r0] "i"(0), [r1] "i"(64), [r2] "i"(128)));
}
DEV void b_stage_345(BState& st, v4u qa0, v4u qa1, v4u msk) {
  asm volatile(B_STAGE3("a", "v", "v") B_OPERANDS([r0] "i"(192), [r1] "i"(64), [r2] "i"(128)));
}

#define A_RD(BK, XOR, S16) \
  "v_xor_b32 %[t], " #XOR ", %[kb]\n\t" \
  "ds_read_b128 v[" #BK ":" #BK "+3], %[qb] offset:" #S16 "\n\t" \
  "ds_read_b128 v[" #BK "+4:" #BK "+7], %[t]\n\t" \
  "ds_read_b128 v[" #BK "+8:" #BK "+11], %[t] offset:4096\n\t" \
  "ds_read_b128 v[" #BK "+12:" #BK "+15], %[t] offset:8192\n\t" \
  "ds_read_b128 v[" #BK "+16:" #BK "+19], %[t] offset:12288\n\t"
#define A_MM(BK, C0, C1, C2, C3) \
  MF "v[8:11], v[" #BK ":" #BK "+3], v[" #BK "+4:" #BK "+7], " C0 "\n\t" \
  MF "v[12:15], v[" #BK ":" #BK "+3], v[" #BK "+8:" #BK "+11], " C1 "\n\t" \
  MF "v[16:19], v[" #BK ":" #BK "+3], v[" #BK "+12:" #BK "+15], " C2 "\n\t" \
  MF "v[20:23], v[" #BK ":" #BK "+3], v[" #BK "+16:" #BK "+19], " C3 "\n\t"
#define A_SEL(O, H) \
  "v_cndmask_b32_e64 " O ", v[8+" #H "], v[12+" #H "], s[84:85]\n\t" \
  "v_cndmask_b32_e64 " O ", " O ", v[16+" #H "], s[86:87]\n\t" \
  "v_cndmask_b32_e64 " O ", " O ", v[20+" #H "], s[88:89]\n\t"
DEV void a_scores(float& s0, float& s1, float& s2, float& s3, int kb, int qb) {
  int t;
  asm volatile(
      "s_waitcnt lgkmcnt(0)\n\t" B_SETUP
      A_RD(24, 0, 0) A_RD(44, 64, 64) "s_waitcnt lgkmcnt(5)\n\t" A_MM(24, "0", "0", "0", "0")
      A_RD(24, 128, 128) "s_waitcnt lgkmcnt(5)\n\t" A_MM(44, "v[8:11]", "v[12:15]", "v[16:19]", "v[20:23]")
      A_RD(44, 192, 192) "s_waitcnt lgkmcnt(5)\n\t" A_MM(24, "v[8:11]", "v[12:15]", "v[16:19]", "v[20:23]")
      "s_waitcnt lgkmcnt(0)\n\t" A_MM(44, "v[8:11]", "v[12:15]", "v[16:19]", "v[20:23]")
      "s_nop 7\n\t" A_SEL("%[s0]", 0) A_SEL("%[s1]", 1) A_SEL("%[s2]", 2) A_SEL("%[s3]", 3)
      : [s0] "=&v"(s0), [s1] "=&v"(s1), [s2] "=&v"(s2), [s3] "=&v"(s3), [t] "=&v"(t)
      : [kb] "v"(kb), [qb] "v"(qb)
      : VCLOB_8_31, VCLOB_32_63, SCLOB, ACLOB, "memory");
}
#define A_TR(BK, XOR) \
  "v_xor_b32 %[t], " #XOR ", %[vb]\n\t" \
  "ds_read_b64_tr_b16 v[" #BK ":" #BK "+1], %[t]\n\t" \
  "ds_read_b64_tr_b16 v[" #BK "+2:" #BK "+3], %[t] offset:4096\n\t" \
  "ds_read_b64_tr_b16 v[" #BK "+4:" #BK "+5], %[t] offset:8192\n\t" \
  "ds_read_b64_tr_b16 v[" #BK "+6:" #BK "+7], %[t] offset:12288\n\t"
#define A_PV(BK, D) \
  MF "v[" #D ":" #D "+3], v[" #BK ":" #BK "+3], %[p0], 0\n\t" \
  MF "v[" #D ":" #D "+3], v[" #BK "+4:" #BK "+7], %[p1], v[" #D ":" #D "+3]\n\t"
#define A_ST(D, O) "ds_write_b128 %[cb], v[" #D ":" #D "+3] offset:" #O "\n\t"
DEV float a_pv(v4u p0, v4u p1, int vb, int cb) {
  int t;
  float ls;
  asm volatile(
      "v_mov_b32 v60, 0x3f803f80\n\tv_mov_b32 v61, 0x3f803f80\n\tv_mov_b32 v62, 0x3f803f80\n\tv_mov_b32 v63, 0x3f803f80\n\t"
      "s_waitcnt lgkmcnt(0)\n\t"
      A_TR(16, 0) A_TR(24, 32)
      MF "v[56:59], v[60:63], %[p0], 0\n\t" MF "v[56:59], v[60:63], %[p1], v[56:59]\n\t"
      "s_waitcnt lgkmcnt(4)\n\t" A_PV(16, 32)
      A_TR(16, 64) "s_waitcnt lgkmcnt(4)\n\t" A_PV(24, 36)
      A_TR(24, 96) "s_waitcnt lgkmcnt(4)\n\t" A_PV(16, 40)
      "v_mov_b32 %[ls], v56\n\t"
      A_TR(16, 128) "s_waitcnt lgkmcnt(4)\n\t" A_PV(24, 44)
      A_TR(24, 160) "s_waitcnt lgkmcnt(4)\n\t" A_PV(16, 48)
      A_TR(16, 192) "s_waitcnt lgkmcnt(4)\n\t" A_PV(24, 52)
      A_TR(24, 224) "s_waitcnt lgkmcnt(4)\n\t" A_PV(16, 56)
      "s_waitcnt lgkmcnt(0)\n\t" A_PV(24, 60)
      "s_mov_b64 s[84:85], exec\n\ts_mov_b32 s86, 0x000f000f\n\ts_mov_b32 s87, 0x000f000f\n\ts_mov_b64 exec, s[86:87]\n\t"
      A_ST(32, 0) A_ST(36, 64) A_ST(40, 128) A_ST(44, 192) A_ST(48, 256) A_ST(52, 320) "s_nop 3\n\t" A_ST(56, 384) A_ST(60, 448)
      "s_mov_b64 exec, s[84:85]"
      : [t] "=&v"(t), [ls] "=&v"(ls)
      : [p0] "v"(p0), [p1] "v"(p1), [vb] "v"(vb), [cb] "v"(cb)
      : VCLOB_16_31, VCLOB_32_63, SCLOB, ACLOB, "memory");
  return ls;
}

DEV void dma_a(const Params& p, int b, int team, int j, int w, int lane, int q) {
  const size_t row0 = ((size_t)(b * 8 + team) * SK + 256 * j + 64 * w) * 128;
  const v4u sk = make_srd(p.kA + row0, 16384), sv = make_srd(p.vA + row0, 16384);
  const int r = lane >> 4, c = lane & 15;
  const int pk = r * 256 + ((c ^ r) << 4), pv = r * 256 + ((c ^ (2 * r)) << 4);
  unsigned keep;
  const int mk = __builtin_amdgcn_readfirstlane(L_AK + w * 16384 + q * 4096), mv = mk + (L_AV - L_AK), so = q * 4096;
  asm volatile(
      "s_mov_b32 %0, m0\n\ts_mov_b32 m0, %1\n\ts_nop 4\n\t"
      "buffer_load_dwordx4 %2, %6, %7 offen nt lds\n\t"
      "buffer_load_dwordx4 %3, %6, %7 offen offset:1024 nt lds\n\t"
      "buffer_load_dwordx4 %4, %6, %7 offen offset:2048 nt lds\n\t"
      "buffer_load_dwordx4 %5, %6, %7 offen offset:3072 nt lds\n\t"
      "s_mov_b32 m0, %0"
      : "=&s"(keep) : "s"(mk), "v"(pk), "v"(pk ^ 64), "v"(pk ^ 128), "v"(pk ^ 192), "s"(sk), "s"(so) : "memory");
  asm volatile(
      "s_mov_b32 %0, m0\n\ts_mov_b32 m0, %1\n\ts_nop 4\n\t"
      "buffer_load_dwordx4 %2, %4, %5 offen nt lds\n\t"
      "buffer_load_dwordx4 %3, %4, %5 offen offset:1024 nt lds\n\t"
      "buffer_load_dwordx4 %2, %4, %5 offen offset:2048 nt lds\n\t"
      "buffer_load_dwordx4 %3, %4, %5 offen offset:3072 nt lds\n\t"
      "s_mov_b32 m0, %0"
      : "=&s"(keep) : "s"(mv), "v"(pv), "v"(pv ^ 128), "s"(sv), "s"(so) : "memory");
}

template <int LO = 0, int HI = 8> DEV void load_w(v4u (&wv)[8], const u16* wbase, int ld, int tid) {
  const rsrc_t r = __builtin_amdgcn_make_buffer_rsrc((void*)wbase, 0, 512 * ld * 2, 0x00020000);
  const int off = (8 * (tid >> 2) * ld + 8 * (tid & 3)) * 2;
#pragma unroll
  for (int u = LO; u < HI; ++u) wv[u] = __builtin_amdgcn_raw_buffer_load_b128(r, off, u * ld * 2, 0);
}
DEV float dot2(unsigned a, unsigned b, float c) {
  typedef __bf16 bf16x2 __attribute__((ext_vector_type(2)));
  return __builtin_amdgcn_fdot2_f32_bf16(__builtin_bit_cast(bf16x2, a), __builtin_bit_cast(bf16x2, b), c, false);
}
DEV float gemv32(const v4u (&wv)[8], v4u a, LAS char* L, int tid) {
  float acc[8];
#pragma unroll
  for (int i = 0; i < 8; ++i) acc[i] = 0.f;
#pragma unroll
  for (int u = 0; u < 8; ++u) {
    const unsigned aw = a[u >> 1];
    const unsigned al = (u & 1) ? aw >> 16 : aw & 0xffffu, ah = (u & 1) ? aw & 0xffff0000u : aw << 16;
    acc[0] = dot2(wv[u].x, al, acc[0]); acc[1] = dot2(wv[u].x, ah, acc[1]);
    acc[2] = dot2(wv[u].y, al, acc[2]); acc[3] = dot2(wv[u].y, ah, acc[3]);
    acc[4] = dot2(wv[u].z, al, acc[4]); acc[5] = dot2(wv[u].z, ah, acc[5]);
    acc[6] = dot2(wv[u].w, al, acc[6]); acc[7] = dot2(wv[u].w, ah, acc[7]);
  }
#pragma unroll
  for (int i = 0; i < 8; ++i) acc[i] += dppf<0x124>(acc[i]);
#pragma unroll
  for (int i = 0; i < 8; ++i) acc[i] += dppf<0x128>(acc[i]);
  if ((tid & 15) < 4) {
    LAS f32x4* d = (LAS f32x4*)(L + L_RED + (tid >> 4) * 128 + (tid & 3) * 32);
    d[0] = f32x4{acc[0], acc[1], acc[2], acc[3]}; d[1] = f32x4{acc[4], acc[5], acc[6], acc[7]};
  }
  lds_barrier();
  float s = 0.f;
  if (tid < 64) {
    const LAS float* r = (const LAS float*)(L + L_RED) + (tid >> 5) * 256 + (tid & 31);
    s = ((r[0] + r[32]) + (r[64] + r[96])) + ((r[128] + r[160]) + (r[192] + r[224]));
    const auto sw = __builtin_amdgcn_permlane32_swap(asu(s), asu(s), false, false);
    s = asf(sw[0]) + asf(sw[1]);
  }
  return s;
}
DEV v4u ln_stage(unsigned xn, LAS char* L, int tid, int stage) {
  const float a = lo(xn), b = hi(xn);
  const float s1 = wave_sum(a + b), s2 = wave_sum(a * a + b * b);
  *(LAS unsigned*)(L + stage + tid * 4) = xn;
  if ((tid & 63) == 0) *(LAS f32x2*)(L + L_ST + (tid >> 6) * 8) = f32x2{s1, s2};
  lds_barrier();
  const f32x4 t0 = *(const LAS f32x4*)(L + L_ST), t1 = *(const LAS f32x4*)(L + L_ST + 16);
  const v4u xv = *(const LAS v4u*)(L + stage + (tid >> 2) * 16);
  const float mean = ((t0.x + t0.z) + (t1.x + t1.z)) * (1.f / DM);
  const float var = fmaxf(((t0.y + t0.w) + (t1.y + t1.w)) * (1.f / DM) - mean * mean, 0.f);
  const float mb = rbf(mean), rd = __builtin_amdgcn_rcpf(rbf(__builtin_amdgcn_sqrtf(rbf(rbf(var) + 1e-5f))));
  v4u o;
#pragma unroll
  for (int i = 0; i < 4; ++i) o[i] = f2bf(rbf(lo(xv[i]) - mb) * rd) | (f2bf(rbf(hi(xv[i]) - mb) * rd) << 16);
  return o;
}

enum { T_Q = 0, T_A = 1, T_B = 2, T_H1 = 3, T_H2 = 4 };
#define OP(T, K, DROW, NBURST, NDMA) ((unsigned long long)((T) | ((K) << 3) | (((DROW) + 1) << 5) | ((NBURST) << 7) | ((NDMA) << 10)))
constexpr int NOPS = 16, PC0 = 10;
#define NBB 1
constexpr unsigned long long prog[NOPS] = {
    OP(T_Q, 0, 0, NB0, 0),
    OP(T_H2, 3, -1, 0, 0),
    OP(T_B, 1, 0, 0, 0),
    OP(T_H1, 1, 0, 0, 0),
    OP(T_H2, 1, 0, 0, 0),
    OP(T_Q, 1, 0, 0, 0),
    OP(T_A, 2, 0, 0, 1),
    OP(T_H1, 2, 0, 0, 1),
    OP(T_H2, 2, 0, 0, 1),
    OP(T_Q, 2, 0, 0, 0),
    OP(T_B, 3, 0, NBB, 0),
    OP(T_Q, 3, 1, NB1, 0),
    OP(T_A, 0, 1, NB2, 0),
    OP(T_H1, 3, 0, NB3, 0),
    OP(T_H1, 0, 1, NB4, 0),
    OP(T_H2, 0, 1, NB5, 0),
};
constexpr unsigned long long pack4(int i) { return prog[i] | (prog[i + 1] << 16) | (prog[i + 2] << 32) | (prog[i + 3] << 48); }

__global__ void __launch_bounds__(256, 1) __attribute__((amdgpu_num_vgpr(64))) mega_kernel(Params p) {
  extern __shared__ __attribute__((aligned(16))) char smem[];
  asm volatile("" ::: "v255", "a255");
  LAS char* L = (LAS char*)smem;
  const int w_ = __builtin_amdgcn_readfirstlane(threadIdx.x >> 6), bid_ = blockIdx.x;
  const int grp = (bid_ >> 3) & 1;
  const bool writer = (bid_ & 7) == 0;
  const rsrc_t rs = __builtin_amdgcn_make_buffer_rsrc((void*)(p.ws + (size_t)grp * W_GROUP), 0, W_GROUP * 4, 0x00020000);
#define IDS() \
  const int lane = lane_id(), w = fresh_s(w_), tid = w * 64 + lane, n16 = lane & 15, g4 = lane >> 4, bid = fresh_s(bid_), team = bid & 7, j = bid >> 4; \
  (void)tid; (void)lane; (void)n16; (void)g4; (void)w; (void)team; (void)j
#define STREAM_ARGS(R) \
  const int lk = (n16 >> 1) * 256 + (n16 & 1) * 64 + g4 * 16, lv = g4 * 2048 + n16 * 16; \
  const size_t srow = ((size_t)((grp + 2 * (R)) * 32 + 4 * team + (j >> 2)) * SK + 1024 * (j & 3) + 256 * w) * 128; \
  const v4u skB = make_srd(p.kB + srow, 65536), svB = make_srd(p.vB + srow, 65536)
#define TICK() do { \
    if (ndma != 0) { if (row < 3 && nq < 4 * (row + 1)) { IDS(); dma_a(p, grp + 2 * (row + 1), team, j, w, lane, nq & 3); ++nq; } } \
    else if (nburst != 0 && tok != 0u) { IDS(); STREAM_ARGS(strow); \
      for (int k_ = 0; k_ < nburst && nb < NHB; ++k_) { half_burst(nb, lk, lv, skB, svB); ++nb; } } } while (0)
#define SETTLE() __builtin_amdgcn_s_waitcnt(0x0f70)
#define SEND_TOK() do { if (NOTOK != 1 && w == 0 && row + grp < (NOTOK == 2 ? 1 : 4)) { \
    const rsrc_t rso = __builtin_amdgcn_make_buffer_rsrc((void*)(p.ws + (size_t)(grp ^ 1) * W_GROUP), 0, W_GROUP * 4, 0x00020000); \
    st1(rso, (W_TOK + (team * 16 + j) * 16 + row + grp) * 4, 1u); } } while (0)
#define DMA_B2() do { if (!first && row < 3 && nq < 4 * (row + 1)) { IDS(); dma_a(p, grp + 2 * (row + 1), team, j, w, lane, nq & 3); ++nq; } } while (0)
#define CATCH_UP(N) do { while (nb < (N)) { half_burst(nb, lk, lv, skB, svB); ++nb; } } while (0)
#define TOKOFF ((W_TOK + (team * 16 + j) * 16 + strow) * 4)
#define POLL_TOK() do { if (NOTOK != 1 && tok == 0u && nburst != 0) tok = __builtin_amdgcn_readfirstlane(tok | ld1(rs, TOKOFF)); } while (0)

  int nb = 0;
  unsigned tok = (NOTOK == 1 || grp == 0) ? 1u : 0u;
  int stamp_i = 0; (void)stamp_i;
  STAMP(0);
  {
    v4u wv[8];
    { IDS(); load_w(wv, p.wqA + 512 * team + 32 * j, QD, tid); }
    for (int r = 0; r < 4; ++r) {
      IDS();
      const unsigned xr = *(const unsigned*)(p.x + (grp + 2 * r) * DM + 2 * tid);
      dma_a(p, grp, team, j, w, lane, r);
      const v4u a = ln_stage(xr, L, tid, L_O);
      const float q = gemv32(wv, a, L, tid);
      if (tid < 32) st1(rs, (W_Q + (3 + r) * QD + 512 * team + 32 * j + tid) * 4, (f2bf(q) << 16) | (1u + (unsigned)r % 3u));
    }
  }
  int nq = 0;

  for (int pc = 11 - NOPS; pc < 4 * NOPS + 2; ++pc) {
    const int it = (pc >> 4), idx = pc & 15;
    const unsigned long long pw = (idx & 8) ? ((idx & 4) ? pack4(12) : pack4(8)) : ((idx & 4) ? pack4(4) : pack4(0));
    const unsigned op = (unsigned)(pw >> (16 * (idx & 3))) & 0xffffu;
    const int type = op & 7, kind = (op >> 3) & 3, row = it + (int)((op >> 5) & 3) - 1, ndma = (op >> 10) & 7;
    const int strow = idx >= PC0 ? it + 1 : it;
    const int nburst = strow < 4 ? (op >> 7) & 7 : 0;
    if (row < 0 || row > 3) continue;
    tok = __builtin_amdgcn_readfirstlane(tok); nb = __builtin_amdgcn_readfirstlane(nb); nq = __builtin_amdgcn_readfirstlane(nq);
    const unsigned tag = 1u + (unsigned)row % 3u, ptag = (tag & 1u) | ((tag & 2u) << 15);
    const bool isA = (kind & 1) == 0;

    if (type == T_Q) {
      IDS();
      const bool forB = (kind & 1) == 0;
      if (!forB || w == 0) {
        const int qoff = W_Q + (kind == 3 ? 3 + row : kind) * QD + 512 * team + (forB ? 128 * (j >> 2) : 0);
        unsigned spins = 0;
        v2u qv = ld2(rs, tid * 8, qoff * 4);
        for (;;) {
          cfence();
          const v2u qn = ld2(rs, tid * 8, qoff * 4);
          if (none((((qv.x ^ tag) | (qv.y ^ tag)) & 3u) != 0u) || ++spins > SPIN_CAP) break;
          POLL_TOK();
          qv = qn;
        }
        *(LAS unsigned*)(L + L_Q + tid * 4) = (qv.x >> 16) | (qv.y & 0xffff0000u);
      }
      lds_barrier();
      SETTLE(); TICK();
    } else if (type == T_A || type == T_B) {
      if (type == T_A) {
        float sc[4];
        { IDS(); a_scores(sc[0], sc[1], sc[2], sc[3], L_AK + (64 * w + n16) * 256 + ((g4 ^ n16) << 4), L_Q + (n16 & 3) * 256 + g4 * 16); }
        SUB(0);
        if (kind == 0) TICK();
        IDS();
        LAS float* comb = (LAS float*)(L + L_U) + w * 4 * CREC;
        LAS u16* pex = (LAS u16*)comb;
        const int ppos = 32 * (g4 >> 1) + 8 * (n16 >> 2) + 4 * (g4 & 1) + (n16 & 3);
        float mh[4];
#pragma unroll
        for (int h = 0; h < 4; ++h) { sc[h] = rbf(rbf(sc[h]) * SCALE) * LOG2E; mh[h] = wave_max(sc[h]); }
#pragma unroll
        for (int h = 0; h < 4; ++h) pex[h * 64 + ppos] = (u16)f2bf(ex2(sc[h] - mh[h]));
        const v4u pb0 = *(const LAS v4u*)(pex + (n16 & 3) * 64 + 8 * g4), pb1 = *(const LAS v4u*)(pex + (n16 & 3) * 64 + 32 + 8 * g4);
        const float mm = lane == 0 ? mh[0] : lane == 1 ? mh[1] : lane == 2 ? mh[2] : mh[3];
        SUB(1);
        const int q_ = (lane >> 2) & 3, p_ = lane & 3;
        const float ll = a_pv(pb0, pb1, L_AV + (64 * w + 4 * g4 + q_) * 256 + ((((p_ >> 1) ^ (2 * (4 * (g4 & 1) + q_)))) << 4) + (p_ & 1) * 8,
                              L_U + (w * 4 * CREC + n16 * CREC + 4 * g4) * 4);
        { IDS(); if (lane < 4) *(LAS f32x2*)(L + L_U + ((w * 4 + lane) * CREC + 128) * 4) = f32x2{mm, ll}; }
      } else {
        const bool first = kind == 1;
        if (first) {
          {
            IDS(); STREAM_ARGS(row);
            unsigned spins = 0;
            while (tok == 0u && ++spins < SPIN_CAP) { cfence(); tok = __builtin_amdgcn_readfirstlane(tok | ld1(rs, TOKOFF)); __builtin_amdgcn_s_sleep(2); }
            if (TOK_POS == 0) SEND_TOK();
            CATCH_UP(14);
          }
        }
        v4u qa0 = {0u, 0u, 0u, 0u}, qa1 = {0u, 0u, 0u, 0u}, msk;
        {
          IDS();
          if ((n16 & 2) == 0) {
            qa0 = *(const LAS v4u*)(L + L_Q + (4 * (n16 & 1) + g4) * 16);
            qa1 = *(const LAS v4u*)(L + L_Q + (8 + 4 * (n16 & 1) + g4) * 16);
          }
          const unsigned hm = (n16 & 1) ? 0xffff0000u : 0x0000ffffu;
          const int ri = n16 >> 1;
          msk.x = ri == 0 ? hm : 0u; msk.y = ri == 1 ? hm : 0u; msk.z = ri == 2 ? hm : 0u; msk.w = ri == 3 ? hm : 0u;
          if (first) wait_vm<63>();
        }
        BState st; st.acc = f32x4{0.f, 0.f, 0.f, 0.f}; st.m = -INFINITY; st.l = 0.f;
        b_stage_last(st, qa0, qa1, msk);
        SUB(0);
        {
          IDS(); STREAM_ARGS(row);
          const int sv_off = L_RB + w * (NSAVE * 1024) + lane * 16;
          if (first) {
            asm volatile("ds_write_b128 %0, v[236:239]\n\tds_write_b128 %0, v[240:243] offset:1024\n\tds_write_b128 %0, v[244:247] offset:2048\n\t"
                         "ds_write_b128 %0, v[248:251] offset:3072\n\tds_write_b128 %0, v[252:255] offset:4096\n\ts_waitcnt lgkmcnt(0)" ::"v"(sv_off) : "memory");
            if (TOK_POS == 1) SEND_TOK();
            ld_unit<6, 0, 16>(lk, lv, skB, svB, 7);
            if (TOK_POS == 2) SEND_TOK();
            wait_vm<63>();
          } else {
            ld_unit<6, 0, 16 - NSAVE>(lk, lv, skB, svB, 0);
            asm volatile("ds_read_b128 v[236:239], %0\n\tds_read_b128 v[240:243], %0 offset:1024\n\tds_read_b128 v[244:247], %0 offset:2048\n\t"
                         "ds_read_b128 v[248:251], %0 offset:3072\n\tds_read_b128 v[252:255], %0 offset:4096" ::"v"(sv_off) : "memory");
          }
        }
        const int nq0 = nq;
        DMA_B2(); DMA_B2();
        b_stage_012(st, qa0, qa1, msk);
        if (first) wait_vm<16>();
        SUB(1);
        DMA_B2();
        if (TOK_POS == 3 && first) { IDS(); SEND_TOK(); }
        b_stage_345(st, qa0, qa1, msk);
        if (nq - nq0 == 3) wait_vm<24>(); else wait_vm<0>();
        asm volatile("s_waitcnt lgkmcnt(0)" ::: "memory");
        DMA_B2();
        if (TOK_POS == 4 && first) { IDS(); SEND_TOK(); }
        b_stage_last(st, qa0, qa1, msk);
        {
          IDS();
          LAS float* comb = (LAS float*)(L + L_U) + w * 4 * CREC;
          const float lsum = wave_sum(st.l) * 0.5f;
          if (n16 < 8) {
            comb[32 * g4 + n16] = st.acc.x; comb[32 * g4 + 8 + n16] = st.acc.y; comb[32 * g4 + 16 + n16] = st.acc.z; comb[32 * g4 + 24 + n16] = st.acc.w;
          }
          if (lane == 0) *(LAS f32x2*)(comb + 128) = f32x2{st.m, lsum};
        }
        if (!first) { nb = 0; tok = NOTOK ? 1u : 0u; }
      }
      SUB(2);
      lds_barrier();
      SUB(3);
      {
        IDS();
        const bool isml = tid >= 128;
        const int h = isml ? tid - 128 : isA ? tid >> 4 : 0;
        if (isml ? h < (isA ? 4 : 1) : tid < (isA ? 64 : 32)) {
          const LAS float* cb = (const LAS float*)(L + L_U) + h * CREC;
          const f32x2 a0 = *(const LAS f32x2*)(cb + 128), a1 = *(const LAS f32x2*)(cb + 4 * CREC + 128), a2 = *(const LAS f32x2*)(cb + 8 * CREC + 128),
                      a3 = *(const LAS f32x2*)(cb + 12 * CREC + 128);
          const float M = fmaxf(fmaxf(a0.x, a1.x), fmaxf(a2.x, a3.x));
          const float f0 = ex2(a0.x - M), f1 = ex2(a1.x - M), f2 = ex2(a2.x - M), f3 = ex2(a3.x - M);
          const int rec = isA ? W_P1A + (kind >> 1) * SZ_P1A + ((team * 16 + j) * 4 + h) * RECA : W_P1B + (kind >> 1) * SZ_P1B + (team * 16 + j) * RECB;
          if (isml) {
            st2(rs, (rec + (isA ? 64 : 128)) * 4, v2u{tagw(M, tag), tagw((f0 * a0.y + f1 * a1.y) + (f2 * a2.y + f3 * a3.y), tag)});
          } else if (isA) {
            const int d = 8 * (tid & 15);
            const f32x4 o = (*(const LAS f32x4*)(cb + d) * f0 + *(const LAS f32x4*)(cb + 4 * CREC + d) * f1) +
                            (*(const LAS f32x4*)(cb + 8 * CREC + d) * f2 + *(const LAS f32x4*)(cb + 12 * CREC + d) * f3);
            const f32x4 q = (*(const LAS f32x4*)(cb + d + 4) * f0 + *(const LAS f32x4*)(cb + 4 * CREC + d + 4) * f1) +
                            (*(const LAS f32x4*)(cb + 8 * CREC + d + 4) * f2 + *(const LAS f32x4*)(cb + 12 * CREC + d + 4) * f3);
            st4(rs, (rec + 4 * (tid & 15)) * 4, v4u{tagp(o.x, o.y, ptag), tagp(o.z, o.w, ptag), tagp(q.x, q.y, ptag), tagp(q.z, q.w, ptag)});
          } else {
            const int d = 4 * tid;
            const f32x4 o = (*(const LAS f32x4*)(cb + d) * f0 + *(const LAS f32x4*)(cb + 4 * CREC + d) * f1) +
                            (*(const LAS f32x4*)(cb + 8 * CREC + d) * f2 + *(const LAS f32x4*)(cb + 12 * CREC + d) * f3);
            st4(rs, (rec + d) * 4, v4u{tagw(o.x, tag), tagw(o.y, tag), tagw(o.z, tag), tagw(o.w, tag)});
          }
        }
      }
      TICK();
    } else if (type == T_H1) {
      v4u wv[8];
      unsigned pr;
      { IDS(); load_w<0, 6>(wv, (isA ? p.woA : p.woB) + (size_t)512 * team * DM + 32 * j, DM, tid); }
      {
        IDS();
        unsigned spins = 0;
        float o0 = 0.f, o1 = 0.f;
        if (isA) {
          const int base = (W_P1A + (kind >> 1) * SZ_P1A + (team * 64 + w) * RECA) * 4;
          v2u ml; unsigned o[16];
          for (;;) {
            cfence();
            ml = ld2(rs, n16 * (4 * RECA * 4) + 256, base);
#pragma unroll
            for (int s = 0; s < 16; ++s) o[s] = ld1(rs, lane * 4, base + s * (4 * RECA * 4));
            POLL_TOK();
            unsigned aa = o[0], oo = o[0];
#pragma unroll
            for (int s = 1; s < 16; ++s) { aa &= o[s]; oo |= o[s]; }
            const unsigned bad = (((aa ^ ptag) | (oo ^ ptag)) & 0x10001u) | (((ml.x ^ tag) | (ml.y ^ tag)) & 3u);
            if (none(bad != 0u) || ++spins > SPIN_CAP) break;
          }
          SUB(0);
          cfence();
          load_w<6, 8>(wv, p.woA + (size_t)512 * team * DM + 32 * j, DM, tid);
          const float m = asf(ml.x), M = row_max(m), wt = ex2(m - M);
          const float f = wt * __builtin_amdgcn_rcpf(row_sum(wt * asf(ml.y)));
#pragma unroll
          for (int s = 0; s < 16; ++s) { const float fs = rdl(f, s); o0 = fmaf(fs, lo(o[s]), o0); o1 = fmaf(fs, hi(o[s]), o1); }
        } else {
          const int base = (W_P1B + (kind >> 1) * SZ_P1B + (team * 16 + 4 * w) * RECB) * 4;
          load_w<6, 8>(wv, p.woB + (size_t)512 * team * DM + 32 * j, DM, tid);
          v2u ml, o[4];
          for (;;) {
            cfence();
            ml = ld2(rs, (lane & 3) * (RECB * 4) + 512, base);
#pragma unroll
            for (int s = 0; s < 4; ++s) o[s] = ld2(rs, lane * 8, base + s * (RECB * 4));
            POLL_TOK();
            unsigned aa = ml.x & ml.y, oo = ml.x | ml.y;
#pragma unroll
            for (int s = 0; s < 4; ++s) { aa &= o[s].x & o[s].y; oo |= o[s].x | o[s].y; }
            if (none((((aa ^ tag) | (oo ^ tag)) & 3u) != 0u) || ++spins > SPIN_CAP) break;
          }
          SUB(0);
          float m = asf(ml.x), M = fmaxf(m, dppf<0xB1>(m)); M = fmaxf(M, dppf<0x4E>(M));
          const float wt = ex2(m - M);
          float ls = wt * asf(ml.y); ls += dppf<0xB1>(ls); ls += dppf<0x4E>(ls);
          const float f = wt * __builtin_amdgcn_rcpf(ls);
#pragma unroll
          for (int s = 0; s < 4; ++s) { const float fs = rdl(f, s); o0 = fmaf(fs, asf(o[s].x), o0); o1 = fmaf(fs, asf(o[s].y), o1); }
        }
        pr = f2bf(o0) | (f2bf(o1) << 16);
      }
      SUB(1);
      { IDS(); *(LAS unsigned*)(L + L_O + tid * 4) = pr; }
      SETTLE(); TICK();
      IDS();
      lds_barrier();
      SUB(2);
      const float y = gemv32(wv, *(const LAS v4u*)(L + L_O + (tid >> 2) * 16), L, tid);
      SUB(3);
      if (tid < 32) st1(rs, (W_Y + (kind * 8 + team) * DM + 32 * j + tid) * 4, tagw(y, tag));
      TICK();
    } else if (kind == 3) {
      if (writer && w_ == 0) {
        IDS();
        unsigned yv[8], spins = 0;
        for (;;) {
          cfence();
#pragma unroll
          for (int k = 0; k < 8; ++k) yv[k] = ld1(rs, (lane & 31) * 4, (W_Y + (3 * 8 + k) * DM + 32 * j) * 4);
          unsigned aa = yv[0], oo = yv[0];
#pragma unroll
          for (int k = 1; k < 8; ++k) { aa &= yv[k]; oo |= yv[k]; }
          if (none((((aa ^ tag) | (oo ^ tag)) & 3u) != 0u) || ++spins > SPIN_CAP) break;
        }
        const float ys = ((asf(yv[0]) + asf(yv[1])) + (asf(yv[2]) + asf(yv[3]))) + ((asf(yv[4]) + asf(yv[5])) + (asf(yv[6]) + asf(yv[7])));
        if (lane < 32) p.out[(grp + 2 * row) * DM + 32 * j + lane] = (u16)f2bf(bf2f(*(const LAS u16*)(L + L_XS + lane * 2)) + rbf(ys));
      }
    } else {
      v4u wv[8];
      unsigned xo;
      {
        IDS();
        load_w(wv, (isA ? p.wqB : p.wqA) + 512 * team + 32 * j, QD, tid);
        xo = kind == 0 ? *(const unsigned*)(p.x + (grp + 2 * row) * DM + 2 * tid) : *(const LAS unsigned*)(L + L_XP + tid * 4);
      }
      IDS();
      v2u yv[8];
      unsigned spins = 0;
      for (;;) {
        cfence();
#pragma unroll
        for (int k = 0; k < 8; ++k) yv[k] = ld2(rs, tid * 8, (W_Y + (kind * 8 + k) * DM) * 4);
        POLL_TOK();
        unsigned aa = ~0u, oo = 0u;
#pragma unroll
        for (int k = 0; k < 8; ++k) { aa &= yv[k].x & yv[k].y; oo |= yv[k].x | yv[k].y; }
        if (none((((aa ^ tag) | (oo ^ tag)) & 3u) != 0u) || ++spins > SPIN_CAP) break;
      }
      SUB(0);
      const float y0 = ((asf(yv[0].x) + asf(yv[1].x)) + (asf(yv[2].x) + asf(yv[3].x))) + ((asf(yv[4].x) + asf(yv[5].x)) + (asf(yv[6].x) + asf(yv[7].x)));
      const float y1 = ((asf(yv[0].y) + asf(yv[1].y)) + (asf(yv[2].y) + asf(yv[3].y))) + ((asf(yv[4].y) + asf(yv[5].y)) + (asf(yv[6].y) + asf(yv[7].y)));
      const unsigned xn = f2bf(lo(xo) + rbf(y0)) | (f2bf(hi(xo) + rbf(y1)) << 16);
      if (kind == 2 && (tid >> 4) == j) *(LAS unsigned*)(L + L_XS + (tid & 15) * 4) = xn;
      SETTLE(); TICK();
      SUB(1);
      const v4u a = ln_stage(xn, L, tid, L_XP);
      SUB(2);
      const float q = gemv32(wv, a, L, tid);
      SUB(3);
      if (tid < 32) st1(rs, (W_Q + kind * QD + 512 * team + 32 * j + tid) * 4, (f2bf(q) << 16) | tag);
      TICK();
    }
    stamp_i = pc + NOPS; STAMP(0);
  }
}
}

extern "C" void kernel_launch(void* const* d_in, const int* in_sizes, int n_in, void* d_out, int out_size, void* d_ws, size_t ws_size, hipStream_t stream) {
  typedef const mega::u16* cp;
  mega::Params p;
  p.x = (cp)d_in[0]; p.kA = (cp)d_in[1]; p.vA = (cp)d_in[2]; p.wqA = (cp)d_in[3]; p.woA = (cp)d_in[4];
  p.kB = (cp)d_in[6]; p.vB = (cp)d_in[7]; p.wqB = (cp)d_in[8]; p.woB = (cp)d_in[9];
  p.out = (mega::u16*)d_out; p.ws = (unsigned*)d_ws;
  (void)hipMemsetAsync(d_ws, 0, mega::WS_BYTES, stream);
  (void)hipFuncSetAttribute((const void*)mega::mega_kernel, hipFuncAttributeMaxDynamicSharedMemorySize, 163840);
  mega::mega_kernel<<<256, 256, 163840, stream>>>(p);
}
```

```cpp
#include <hip/hip_runtime.h>
#include <hip/hip_bf16.h>
#include <stdint.h>

namespace mega {
typedef unsigned short u16;
typedef unsigned v4u __attribute__((ext_vector_type(4)));
typedef unsigned v2u __attribute__((ext_vector_type(2)));
typedef float f32x4 __attribute__((ext_vector_type(4)));
typedef float f32x2 __attribute__((ext_vector_type(2)));
#define LAS __attribute__((address_space(3)))
#define DEV __device__ __forceinline__

constexpr int DM = 512, SK = 4096, QD = 4096;
constexpr float SCALE = 0.08838834764831845f, LOG2E = 1.4426950408889634f;
constexpr unsigned SPIN_CAP = 1u << 17;

constexpr int RECA = 68;
constexpr int RECB = 132;
constexpr int SZ_P1A = 8 * 16 * 4 * RECA, SZ_P1B = 8 * 16 * RECB;
constexpr int W_P1A = 0, W_P1B = W_P1A + 2 * SZ_P1A, W_Y = W_P1B + 2 * SZ_P1B, W_Q = W_Y + 4 * 8 * DM,
              W_TOK = W_Q + 7 * QD, W_GROUP = W_TOK + 128 * 16;
constexpr size_t WS_BYTES = (size_t)2 * W_GROUP * 4;

constexpr int CREC = 132;
constexpr int NSAVE = 5;
constexpr int L_AK = 0, L_AV = 65536, L_RB = 131072, L_U = L_RB + 4 * NSAVE * 1024, L_O = L_U + 4 * 4 * CREC * 4,
              L_Q = L_O + 1024, L_XP = L_Q + 1024, L_ST = L_XP + 1024, L_XS = L_ST + 64, L_END = L_XS + 64;
constexpr int L_RED = L_U;
static_assert(L_END <= 163840, "LDS");

struct Params { const u16 *x, *kA, *vA, *wqA, *woA, *kB, *vB, *wqB, *woB; u16* out; unsigned* ws; };

#define ACLOB \
  "a0", "a1", "a2", "a3", "a4", "a5", "a6", "a7", "a8", "a9", "a10", "a11", "a12", "a13", "a14", "a15", \
  "a16", "a17", "a18", "a19", "a20", "a21", "a22", "a23", "a24", "a25", "a26", "a27", "a28", "a29", "a30", "a31", \
  "a32", "a33", "a34", "a35", "a36", "a37", "a38", "a39", "a40", "a41", "a42", "a43", "a44", "a45", "a46", "a47", \
  "a48", "a49", "a50", "a51", "a52", "a53", "a54", "a55", "a56", "a57", "a58", "a59", "a60", "a61", "a62", "a63"
#define V8(a, b, c, d, e, f, g, h) "v" #a, "v" #b, "v" #c, "v" #d, "v" #e, "v" #f, "v" #g, "v" #h
#define VCLOB_32_63 V8(32, 33, 34, 35, 36, 37, 38, 39), V8(40, 41, 42, 43, 44, 45, 46, 47), V8(48, 49, 50, 51, 52, 53, 54, 55), V8(56, 57, 58, 59, 60, 61, 62, 63)
#define VCLOB_8_31 V8(8, 9, 10, 11, 12, 13, 14, 15), V8(16, 17, 18, 19, 20, 21, 22, 23), V8(24, 25, 26, 27, 28, 29, 30, 31)
#define VCLOB_16_31 V8(16, 17, 18, 19, 20, 21, 22, 23), V8(24, 25, 26, 27, 28, 29, 30, 31)
#define SCLOB "s84", "s85", "s86", "s87", "s88", "s89", "s90", "s91", "s92", "s93", "s94", "s95"

DEV float bf2f(unsigned u) { return __builtin_bit_cast(float, u << 16); }
DEV unsigned f2bf(float f) { return (unsigned)__builtin_bit_cast(u16, (__bf16)f); }
DEV float rbf(float f) { return bf2f(f2bf(f)); }
DEV float lo(unsigned u) { return __builtin_bit_cast(float, u << 16); }
DEV float hi(unsigned u) { return __builtin_bit_cast(float, u & 0xffff0000u); }
DEV float asf(unsigned u) { return __builtin_bit_cast(float, u); }
DEV unsigned asu(float f) { return __builtin_bit_cast(unsigned, f); }
DEV unsigned tagw(float f, unsigned tag) { return (asu(f) & ~3u) | tag; }
DEV unsigned tagp(float a, float b, unsigned ptag) { return ((f2bf(a) | (f2bf(b) << 16)) & ~0x10001u) | ptag; }
DEV bool none(bool bad) { return __builtin_amdgcn_ballot_w64(bad) == 0ull; }
DEV float ex2(float x) { return __builtin_amdgcn_exp2f(x); }
template <int CTRL> DEV float dppf(float x) { return asf(__builtin_amdgcn_update_dpp(0u, asu(x), CTRL, 0xf, 0xf, true)); }
DEV float rdl(float x, int l) { return asf(__builtin_amdgcn_readlane(asu(x), l)); }
DEV float row_max(float x) {
  x = fmaxf(x, dppf<0xB1>(x)); x = fmaxf(x, dppf<0x4E>(x)); x = fmaxf(x, dppf<0x141>(x)); x = fmaxf(x, dppf<0x140>(x)); return x;
}
DEV float row_sum(float x) {
  x += dppf<0xB1>(x); x += dppf<0x4E>(x); x += dppf<0x141>(x); x += dppf<0x140>(x); return x;
}
template <int CTRL, int RM> DEV float dppk(float x) { return asf(__builtin_amdgcn_update_dpp(asu(x), asu(x), CTRL, RM, 0xf, false)); }
DEV float wave_max(float x) {
  x = row_max(x); x = fmaxf(x, dppk<0x142, 0xa>(x)); x = fmaxf(x, dppk<0x143, 0xc>(x)); return rdl(x, 63);
}
DEV float wave_sum(float x) {
  x = row_sum(x);
  x += asf(__builtin_amdgcn_update_dpp(0u, asu(x), 0x142, 0xa, 0xf, false));
  x += asf(__builtin_amdgcn_update_dpp(0u, asu(x), 0x143, 0xc, 0xf, false));
  return rdl(x, 63);
}
DEV void lds_barrier() { asm volatile("s_waitcnt lgkmcnt(0)\n\ts_barrier" ::: "memory", ACLOB); }
DEV void cfence() { asm volatile("" ::: "memory", ACLOB); }
DEV int lane_id() {
  int l;
  asm volatile("v_mbcnt_lo_u32_b32 %0, -1, 0\n\tv_mbcnt_hi_u32_b32 %0, -1, %0" : "=v"(l)::ACLOB);
  return l;
}
DEV int fresh_s(int x) { asm volatile("" : "+s"(x)); return x; }
template <int N> DEV void wait_vm() { asm volatile("s_waitcnt vmcnt(%c0)" ::"i"(N) : "memory"); }
DEV v4u make_srd(const void* p, unsigned bytes) {
  v4u s; s.x = (unsigned)(size_t)p; s.y = (unsigned)((size_t)p >> 32) & 0xffffu; s.z = bytes; s.w = 0x00020000u; return s;
}
typedef __amdgpu_buffer_rsrc_t rsrc_t;
DEV unsigned ld1(rsrc_t r, int off, int soff = 0) { return __builtin_amdgcn_raw_buffer_load_b32(r, off, soff, 16); }
DEV v2u ld2(rsrc_t r, int off, int soff = 0) { return __builtin_amdgcn_raw_buffer_load_b64(r, off, soff, 16); }
DEV void st1(rsrc_t r, int off, unsigned v) { __builtin_amdgcn_raw_buffer_store_b32(v, r, off, 0, 16); }
DEV void st2(rsrc_t r, int off, v2u v) { __builtin_amdgcn_raw_buffer_store_b64(v, r, off, 0, 16); }
DEV void st4(rsrc_t r, int off, v4u v) { __builtin_amdgcn_raw_buffer_store_b128(v, r, off, 0, 16); }

template <int F, int IMM> DEV void ld_frag(int voff, v4u srd, int soff) {
  if constexpr (F < 64)
    asm volatile("buffer_load_dwordx4 a[%c0:%c0+3], %1, %2, %3 offen offset:%c4 nt" ::"i"(4 * F), "v"(voff), "s"(srd), "s"(soff), "i"(IMM) : "memory");
  else
    asm volatile("buffer_load_dwordx4 v[%c0:%c0+3], %1, %2, %3 offen offset:%c4 nt" ::"i"(64 + 4 * (F - 64)), "v"(voff), "s"(srd), "s"(soff), "i"(IMM) : "memory");
}
template <int U, int I, int HI> DEV void ld_unit_from(int lk, int lv, v4u sk, v4u sv, int s0, int s1) {
  if constexpr (I < HI) {
    if constexpr (I < 8) ld_frag<16 * U + I, ((I >> 1) & 1) * 2048 + (I & 1) * 128>(lk, sk, (I >> 2) ? s1 : s0);
    else ld_frag<16 * U + I, (I - 8) * 256>(lv, sv, s0);
    ld_unit_from<U, I + 1, HI>(lk, lv, sk, sv, s0, s1);
  }
}
template <int U, int LO, int HI> DEV void ld_unit(int lk, int lv, v4u sk, v4u sv, int logical) {
  const int s0 = logical * 8192, s1 = s0 + 4096;
  asm volatile("s_nop 4" ::: "memory");
  ld_unit_from<U, LO, HI>(lk, lv, sk, sv, s0, s1);
}
DEV void half_burst(int h, int lk, int lv, v4u sk, v4u sv) {
  switch (h) {
    case 0: ld_unit<6, 0, 8>(lk, lv, sk, sv, 0); break;
    case 1: ld_unit<6, 8, 16>(lk, lv, sk, sv, 0); break;
    case 2: ld_unit<0, 0, 8>(lk, lv, sk, sv, 1); break;
    case 3: ld_unit<0, 8, 16>(lk, lv, sk, sv, 1); break;
    case 4: ld_unit<1, 0, 8>(lk, lv, sk, sv, 2); break;
    case 5: ld_unit<1, 8, 16>(lk, lv, sk, sv, 2); break;
    case 6: ld_unit<2, 0, 8>(lk, lv, sk, sv, 3); break;
    case 7: ld_unit<2, 8, 16>(lk, lv, sk, sv, 3); break;
    case 8: ld_unit<3, 0, 8>(lk, lv, sk, sv, 4); break;
    case 9: ld_unit<3, 8, 16>(lk, lv, sk, sv, 4); break;
    case 10: ld_unit<4, 0, 8>(lk, lv, sk, sv, 5); break;
    case 11: ld_unit<4, 8, 16>(lk, lv, sk, sv, 5); break;
    case 12: ld_unit<5, 0, 8>(lk, lv, sk, sv, 6); break;
    case 13: ld_unit<5, 8, 16>(lk, lv, sk, sv, 6); break;
    default: break;
  }
}
constexpr int NHB = 14;

#define MF "v_mfma_f32_16x16x32_bf16 "
#define B_SETUP \
  "s_mov_b32 s84, 0xffff0000\n\ts_mov_b32 s85, 0\n\ts_mov_b32 s86, 0\n\ts_mov_b32 s87, 0x0000ffff\n\t" \
  "s_mov_b32 s88, 0\n\ts_mov_b32 s89, 0xffff0000\n\ts_mov_b32 s90, 0xaaaaaaaa\n\ts_mov_b32 s91, 0xaaaaaaaa\n\ts_nop 1\n\t"
#define B_QK(P, R, BK) \
  MF "v[" #BK ":" #BK "+3], %[qa0], " P "[%c[" R "]:%c[" R "]+3], 0\n\t" \
  MF "v[" #BK "+4:" #BK "+7], %[qa0], " P "[%c[" R "]+8:%c[" R "]+11], 0\n\t" \
  MF "v[" #BK "+8:" #BK "+11], %[qa0], " P "[%c[" R "]+16:%c[" R "]+19], 0\n\t" \
  MF "v[" #BK "+12:" #BK "+15], %[qa0], " P "[%c[" R "]+24:%c[" R "]+27], 0\n\t" \
  MF "v[" #BK ":" #BK "+3], %[qa1], " P "[%c[" R "]+4:%c[" R "]+7], v[" #BK ":" #BK "+3]\n\t" \
  MF "v[" #BK "+4:" #BK "+7], %[qa1], " P "[%c[" R "]+12:%c[" R "]+15], v[" #BK "+4:" #BK "+7]\n\t" \
  MF "v[" #BK "+8:" #BK "+11], %[qa1], " P "[%c[" R "]+20:%c[" R "]+23], v[" #BK "+8:" #BK "+11]\n\t" \
  MF "v[" #BK "+12:" #BK "+15], %[qa1], " P "[%c[" R "]+28:%c[" R "]+31], v[" #BK "+12:" #BK "+15]\n\t"
#define B_MERGE(BK, T) \
  "v_cndmask_b32_e64 v[" #BK "+2], v[" #BK "], v[" #BK "+4], s[84:85]\n\t" \
  "v_cndmask_b32_e64 v[" #BK "+3], v[" #BK "+1], v[" #BK "+5], s[84:85]\n\t" \
  "v_cndmask_b32_e64 v[" #BK "+2], v[" #BK "+2], v[" #BK "+8], s[86:87]\n\t" \
  "v_cndmask_b32_e64 v[" #BK "+3], v[" #BK "+3], v[" #BK "+9], s[86:87]\n\t" \
  "v_cndmask_b32_e64 v[" #BK "+2], v[" #BK "+2], v[" #BK "+12], s[88:89]\n\t" \
  "v_cndmask_b32_e64 v[" #BK "+3], v[" #BK "+3], v[" #BK "+13], s[88:89]\n\t" \
  "v_cndmask_b32_e64 v[" #BK "+2], v[" #BK "+2], v[" #BK "+3], s[90:91]\n\t" \
  "s_nop 1\n\t" \
  "v_add_f32_dpp v[" #BK "+3], v[" #BK "+2], v[" #BK "+2] quad_perm:[1,0,3,2] row_mask:0xf bank_mask:0xf\n\t" \
  "v_cvt_pk_bf16_f32 v[" #BK "+3], v[" #BK "+3], v[" #BK "+3]\n\t" \
  "v_and_b32 v[" #BK "+3], 0xffff0000, v[" #BK "+3]\n\t" \
  "v_mul_f32 v[" #BK "+3], 0x3db504f3, v[" #BK "+3]\n\t" \
  "v_cvt_pk_bf16_f32 v[" #BK "+3], v[" #BK "+3], v[" #BK "+3]\n\t" \
  "v_and_b32 v[" #BK "+3], 0xffff0000, v[" #BK "+3]\n\t" \
  "v_mul_f32 " T ", 0x3fb8aa3b, v[" #BK "+3]\n\t"
#define B_MAX \
  "s_nop 1\n\tv_max_f32_dpp v27, v27, v27 quad_perm:[1,0,3,2] row_mask:0xf bank_mask:0xf\n\t" \
  "s_nop 1\n\tv_max_f32_dpp v27, v27, v27 quad_perm:[2,3,0,1] row_mask:0xf bank_mask:0xf\n\t" \
  "s_nop 1\n\tv_max_f32_dpp v27, v27, v27 row_half_mirror row_mask:0xf bank_mask:0xf\n\t" \
  "s_nop 1\n\tv_max_f32_dpp v27, v27, v27 row_mirror row_mask:0xf bank_mask:0xf\n\t" \
  "s_nop 1\n\t" \
  "v_readlane_b32 s92, v27, 0\n\tv_readlane_b32 s93, v27, 16\n\tv_readlane_b32 s94, v27, 32\n\tv_readlane_b32 s95, v27, 48\n\t" \
  "v_max_f32 v27, s92, %[m]\n\tv_max_f32 v27, s93, v27\n\ts_nop 0\n\tv_max_f32 v27, s94, v27\n\tv_max_f32 v27, s95, v27\n\t" \
  "v_sub_f32 v32, %[m], v27\n\tv_exp_f32 v32, v32\n\tv_mov_b32 %[m], v27\n\t" \
  "v_mul_f32 v28, v28, v32\n\tv_mul_f32 v29, v29, v32\n\tv_mul_f32 v30, v30, v32\n\tv_mul_f32 v31, v31, v32\n\tv_mul_f32 %[l], %[l], v32\n\t"
#define B_EXP(T) "v_sub_f32 " T ", " T ", v27\n\tv_exp_f32 " T ", " T "\n\t"
#define B_ACC(T) "v_add_f32 %[l], %[l], " T "\n\tv_cvt_pk_bf16_f32 " T ", " T ", " T "\n\t"
#define B_AND(S, T, LN) \
  "v_and_b32_dpp v[" #S "], " T ", %[k0] row_newbcast:" #LN " row_mask:0xf bank_mask:0xf\n\t" \
  "v_and_b32_dpp v[" #S "+1], " T ", %[k1] row_newbcast:" #LN " row_mask:0xf bank_mask:0xf\n\t" \
  "v_and_b32_dpp v[" #S "+2], " T ", %[k2] row_newbcast:" #LN " row_mask:0xf bank_mask:0xf\n\t" \
  "v_and_b32_dpp v[" #S "+3], " T ", %[k3] row_newbcast:" #LN " row_mask:0xf bank_mask:0xf\n\t"
#define B_PVM(P, R, O, S) MF "v[28:31], " P "[%c[" R "]+" #O ":%c[" R "]+" #O "+3], v[" #S ":" #S "+3], v[28:31]\n\t"
#define B_PV(P, R, T) \
  B_AND(32, T, 0) B_AND(36, T, 2) B_PVM(P, R, 32, 32) B_AND(40, T, 4) B_PVM(P, R, 36, 36) B_AND(32, T, 6) B_PVM(P, R, 40, 40) \
  B_AND(36, T, 8) B_PVM(P, R, 44, 32) B_AND(40, T, 10) B_PVM(P, R, 48, 36) B_AND(32, T, 12) B_PVM(P, R, 52, 40) \
  B_AND(36, T, 14) B_PVM(P, R, 56, 32) "s_nop 1\n\t" B_PVM(P, R, 60, 36)

struct BState { f32x4 acc; float m, l; };
#define B_OPERANDS(...) \
  : "+{v[28:31]}"(st.acc), [m] "+v"(st.m), [l] "+v"(st.l) \
  : [qa0] "v"(qa0), [qa1] "v"(qa1), [k0] "v"(msk.x), [k1] "v"(msk.y), [k2] "v"(msk.z), [k3] "v"(msk.w), __VA_ARGS__ \
  : "v24", "v25", "v26", "v27", VCLOB_32_63, SCLOB, ACLOB, "memory"
DEV void b_stage_last(BState& st, v4u qa0, v4u qa1, v4u msk) {
  asm volatile(B_SETUP B_QK("v", "r0", 32) "s_nop 7\n\t" B_MERGE(32, "v24") "v_mov_b32 v27, v24\n\t" B_MAX B_EXP("v24") "s_nop 0\n\t" B_ACC("v24") "s_nop 1\n\t"
               B_PV("v", "r0", "v24") "s_nop 7"
               B_OPERANDS([r0] "i"(192)));
}
#define B_STAGE3(P0, P1, P2) \
  B_SETUP B_QK(P0, "r0", 32) B_QK(P1, "r1", 48) B_MERGE(32, "v24") B_QK(P2, "r2", 32) B_MERGE(48, "v25") "s_nop 3\n\t" B_MERGE(32, "v26") \
  "v_max3_f32 v27, v24, v25, v26\n\t" B_MAX B_EXP("v24") B_EXP("v25") B_EXP("v26") "s_nop 0\n\t" B_ACC("v24") B_ACC("v25") B_ACC("v26") "s_nop 1\n\t" \
  B_PV(P0, "r0", "v24") B_PV(P1, "r1", "v25") B_PV(P2, "r2", "v26") "s_nop 7"
DEV void b_stage_012(BState& st, v4u qa0, v4u qa1, v4u msk) {
  asm volatile(B_STAGE3("a", "a", "a") B_OPERANDS([r0] "i"(0), [r1] "i"(64), [r2] "i"(128)));
}
DEV void b_stage_345(BState& st, v4u qa0, v4u qa1, v4u msk) {
  asm volatile(B_STAGE3("a", "v", "v") B_OPERANDS([r0] "i"(192), [r1] "i"(64), [r2] "i"(128)));
}

#define A_RD(BK, XOR, S16) \
  "v_xor_b32 %[t], " #XOR ", %[kb]\n\t" \
  "ds_read_b128 v[" #BK ":" #BK "+3], %[qb] offset:" #S16 "\n\t" \
  "ds_read_b128 v[" #BK "+4:" #BK "+7], %[t]\n\t" \
  "ds_read_b128 v[" #BK "+8:" #BK "+11], %[t] offset:4096\n\t" \
  "ds_read_b128 v[" #BK "+12:" #BK "+15], %[t] offset:8192\n\t" \
  "ds_read_b128 v[" #BK "+16:" #BK "+19], %[t] offset:12288\n\t"
#define A_MM(BK, C0, C1, C2, C3) \
  MF "v[8:11], v[" #BK ":" #BK "+3], v[" #BK "+4:" #BK "+7], " C0 "\n\t" \
  MF "v[12:15], v[" #BK ":" #BK "+3], v[" #BK "+8:" #BK "+11], " C1 "\n\t" \
  MF "v[16:19], v[" #BK ":" #BK "+3], v[" #BK "+12:" #BK "+15], " C2 "\n\t" \
  MF "v[20:23], v[" #BK ":" #BK "+3], v[" #BK "+16:" #BK "+19], " C3 "\n\t"
#define A_SEL(O, H) \
  "v_cndmask_b32_e64 " O ", v[8+" #H "], v[12+" #H "], s[84:85]\n\t" \
  "v_cndmask_b32_e64 " O ", " O ", v[16+" #H "], s[86:87]\n\t" \
  "v_cndmask_b32_e64 " O ", " O ", v[20+" #H "], s[88:89]\n\t"
DEV void a_scores(float& s0, float& s1, float& s2, float& s3, int kb, int qb) {
  int t;
  asm volatile(
      "s_waitcnt lgkmcnt(0)\n\t" B_SETUP
      A_RD(24, 0, 0) A_RD(44, 64, 64) "s_waitcnt lgkmcnt(5)\n\t" A_MM(24, "0", "0", "0", "0")
      A_RD(24, 128, 128) "s_waitcnt lgkmcnt(5)\n\t" A_MM(44, "v[8:11]", "v[12:15]", "v[16:19]", "v[20:23]")
      A_RD(44, 192, 192) "s_waitcnt lgkmcnt(5)\n\t" A_MM(24, "v[8:11]", "v[12:15]", "v[16:19]", "v[20:23]")
      "s_waitcnt lgkmcnt(0)\n\t" A_MM(44, "v[8:11]", "v[12:15]", "v[16:19]", "v[20:23]")
      "s_nop 7\n\t" A_SEL("%[s0]", 0) A_SEL("%[s1]", 1) A_SEL("%[s2]", 2) A_SEL("%[s3]", 3)
      : [s0] "=&v"(s0), [s1] "=&v"(s1), [s2] "=&v"(s2), [s3] "=&v"(s3), [t] "=&v"(t)
      : [kb] "v"(kb), [qb] "v"(qb)
      : VCLOB_8_31, VCLOB_32_63, SCLOB, ACLOB, "memory");
}
#define A_TR(BK, XOR) \
  "v_xor_b32 %[t], " #XOR ", %[vb]\n\t" \
  "ds_read_b64_tr_b16 v[" #BK ":" #BK "+1], %[t]\n\t" \
  "ds_read_b64_tr_b16 v[" #BK "+2:" #BK "+3], %[t] offset:4096\n\t" \
  "ds_read_b64_tr_b16 v[" #BK "+4:" #BK "+5], %[t] offset:8192\n\t" \
  "ds_read_b64_tr_b16 v[" #BK "+6:" #BK "+7], %[t] offset:12288\n\t"
#define A_PV(BK, D) \
  MF "v[" #D ":" #D "+3], v[" #BK ":" #BK "+3], %[p0], 0\n\t" \
  MF "v[" #D ":" #D "+3], v[" #BK "+4:" #BK "+7], %[p1], v[" #D ":" #D "+3]\n\t"
#define A_ST(D, O) "ds_write_b128 %[cb], v[" #D ":" #D "+3] offset:" #O "\n\t"
DEV float a_pv(v4u p0, v4u p1, int vb, int cb) {
  int t;
  float ls;
  asm volatile(
      "v_mov_b32 v60, 0x3f803f80\n\tv_mov_b32 v61, 0x3f803f80\n\tv_mov_b32 v62, 0x3f803f80\n\tv_mov_b32 v63, 0x3f803f80\n\t"
      "s_waitcnt lgkmcnt(0)\n\t"
      A_TR(16, 0) A_TR(24, 32)
      MF "v[56:59], v[60:63], %[p0], 0\n\t" MF "v[56:59], v[60:63], %[p1], v[56:59]\n\t"
      "s_waitcnt lgkmcnt(4)\n\t" A_PV(16, 32)
      A_TR(16, 64) "s_waitcnt lgkmcnt(4)\n\t" A_PV(24, 36)
      A_TR(24, 96) "s_waitcnt lgkmcnt(4)\n\t" A_PV(16, 40)
      "v_mov_b32 %[ls], v56\n\t"
      A_TR(16, 128) "s_waitcnt lgkmcnt(4)\n\t" A_PV(24, 44)
      A_TR(24, 160) "s_waitcnt lgkmcnt(4)\n\t" A_PV(16, 48)
      A_TR(16, 192) "s_waitcnt lgkmcnt(4)\n\t" A_PV(24, 52)
      A_TR(24, 224) "s_waitcnt lgkmcnt(4)\n\t" A_PV(16, 56)
      "s_waitcnt lgkmcnt(0)\n\t" A_PV(24, 60)
      "s_mov_b64 s[84:85], exec\n\ts_mov_b32 s86, 0x000f000f\n\ts_mov_b32 s87, 0x000f000f\n\ts_mov_b64 exec, s[86:87]\n\t"
      A_ST(32, 0) A_ST(36, 64) A_ST(40, 128) A_ST(44, 192) A_ST(48, 256) A_ST(52, 320) "s_nop 3\n\t" A_ST(56, 384) A_ST(60, 448)
      "s_mov_b64 exec, s[84:85]"
      : [t] "=&v"(t), [ls] "=&v"(ls)
      : [p0] "v"(p0), [p1] "v"(p1), [vb] "v"(vb), [cb] "v"(cb)
      : VCLOB_16_31, VCLOB_32_63, SCLOB, ACLOB, "memory");
  return ls;
}

DEV void dma_a(const Params& p, int b, int team, int j, int w, int lane, int q) {
  const size_t row0 = ((size_t)(b * 8 + team) * SK + 256 * j + 64 * w) * 128;
  const v4u sk = make_srd(p.kA + row0, 16384), sv = make_srd(p.vA + row0, 16384);
  const int r = lane >> 4, c = lane & 15;
  const int pk = r * 256 + ((c ^ r) << 4), pv = r * 256 + ((c ^ (2 * r)) << 4);
  unsigned keep;
  const int mk = __builtin_amdgcn_readfirstlane(L_AK + w * 16384 + q * 4096), mv = mk + (L_AV - L_AK), so = q * 4096;
  asm volatile(
      "s_mov_b32 %0, m0\n\ts_mov_b32 m0, %1\n\ts_nop 4\n\t"
      "buffer_load_dwordx4 %2, %6, %7 offen nt lds\n\t"
      "buffer_load_dwordx4 %3, %6, %7 offen offset:1024 nt lds\n\t"
      "buffer_load_dwordx4 %4, %6, %7 offen offset:2048 nt lds\n\t"
      "buffer_load_dwordx4 %5, %6, %7 offen offset:3072 nt lds\n\t"
      "s_mov_b32 m0, %0"
      : "=&s"(keep) : "s"(mk), "v"(pk), "v"(pk ^ 64), "v"(pk ^ 128), "v"(pk ^ 192), "s"(sk), "s"(so) : "memory");
  asm volatile(
      "s_mov_b32 %0, m0\n\ts_mov_b32 m0, %1\n\ts_nop 4\n\t"
      "buffer_load_dwordx4 %2, %4, %5 offen nt lds\n\t"
      "buffer_load_dwordx4 %3, %4, %5 offen offset:1024 nt lds\n\t"
      "buffer_load_dwordx4 %2, %4, %5 offen offset:2048 nt lds\n\t"
      "buffer_load_dwordx4 %3, %4, %5 offen offset:3072 nt lds\n\t"
      "s_mov_b32 m0, %0"
      : "=&s"(keep) : "s"(mv), "v"(pv), "v"(pv ^ 128), "s"(sv), "s"(so) : "memory");
}

template <int LO = 0, int HI = 8> DEV void load_w(v4u (&wv)[8], const u16* wbase, int ld, int tid) {
  const rsrc_t r = __builtin_amdgcn_make_buffer_rsrc((void*)wbase, 0, 512 * ld * 2, 0x00020000);
  const int off = (8 * (tid >> 2) * ld + 8 * (tid & 3)) * 2;
#pragma unroll
  for (int u = LO; u < HI; ++u) wv[u] = __builtin_amdgcn_raw_buffer_load_b128(r, off, u * ld * 2, 0);
}
DEV float dot2(unsigned a, unsigned b, float c) {
  typedef __bf16 bf16x2 __attribute__((ext_vector_type(2)));
  return __builtin_amdgcn_fdot2_f32_bf16(__builtin_bit_cast(bf16x2, a), __builtin_bit_cast(bf16x2, b), c, false);
}
DEV float gemv32(const v4u (&wv)[8], v4u a, LAS char* L, int tid) {
  float acc[8];
#pragma unroll
  for (int i = 0; i < 8; ++i) acc[i] = 0.f;
#pragma unroll
  for (int u = 0; u < 8; ++u) {
    const unsigned aw = a[u >> 1];
    const unsigned al = (u & 1) ? aw >> 16 : aw & 0xffffu, ah = (u & 1) ? aw & 0xffff0000u : aw << 16;
    acc[0] = dot2(wv[u].x, al, acc[0]); acc[1] = dot2(wv[u].x, ah, acc[1]);
    acc[2] = dot2(wv[u].y, al, acc[2]); acc[3] = dot2(wv[u].y, ah, acc[3]);
    acc[4] = dot2(wv[u].z, al, acc[4]); acc[5] = dot2(wv[u].z, ah, acc[5]);
    acc[6] = dot2(wv[u].w, al, acc[6]); acc[7] = dot2(wv[u].w, ah, acc[7]);
  }
#pragma unroll
  for (int i = 0; i < 8; ++i) acc[i] += dppf<0x124>(acc[i]);
#pragma unroll
  for (int i = 0; i < 8; ++i) acc[i] += dppf<0x128>(acc[i]);
  if ((tid & 15) < 4) {
    LAS f32x4* d = (LAS f32x4*)(L + L_RED + (tid >> 4) * 128 + (tid & 3) * 32);
    d[0] = f32x4{acc[0], acc[1], acc[2], acc[3]}; d[1] = f32x4{acc[4], acc[5], acc[6], acc[7]};
  }
  lds_barrier();
  float s = 0.f;
  if (tid < 64) {
    const LAS float* r = (const LAS float*)(L + L_RED) + (tid >> 5) * 256 + (tid & 31);
    s = ((r[0] + r[32]) + (r[64] + r[96])) + ((r[128] + r[160]) + (r[192] + r[224]));
    const auto sw = __builtin_amdgcn_permlane32_swap(asu(s), asu(s), false, false);
    s = asf(sw[0]) + asf(sw[1]);
  }
  return s;
}
DEV v4u ln_stage(unsigned xn, LAS char* L, int tid, int stage) {
  const float a = lo(xn), b = hi(xn);
  const float s1 = wave_sum(a + b), s2 = wave_sum(a * a + b * b);
  *(LAS unsigned*)(L + stage + tid * 4) = xn;
  if ((tid & 63) == 0) *(LAS f32x2*)(L + L_ST + (tid >> 6) * 8) = f32x2{s1, s2};
  lds_barrier();
  const f32x4 t0 = *(const LAS f32x4*)(L + L_ST), t1 = *(const LAS f32x4*)(L + L_ST + 16);
  const v4u xv = *(const LAS v4u*)(L + stage + (tid >> 2) * 16);
  const float mean = ((t0.x + t0.z) + (t1.x + t1.z)) * (1.f / DM);
  const float var = fmaxf(((t0.y + t0.w) + (t1.y + t1.w)) * (1.f / DM) - mean * mean, 0.f);
  const float mb = rbf(mean), rd = __builtin_amdgcn_rcpf(rbf(__builtin_amdgcn_sqrtf(rbf(rbf(var) + 1e-5f))));
  v4u o;
#pragma unroll
  for (int i = 0; i < 4; ++i) o[i] = f2bf(rbf(lo(xv[i]) - mb) * rd) | (f2bf(rbf(hi(xv[i]) - mb) * rd) << 16);
  return o;
}

enum { T_Q = 0, T_A = 1, T_B = 2, T_H1 = 3, T_H2 = 4 };
#define OP(T, K, DROW, NBURST, NDMA) ((unsigned long long)((T) | ((K) << 3) | (((DROW) + 1) << 5) | ((NBURST) << 7) | ((NDMA) << 10)))
constexpr int NOPS = 16, PC0 = 10;
constexpr unsigned long long prog[NOPS] = {
    OP(T_Q, 0, 0, 1, 0),
    OP(T_H2, 3, -1, 0, 0),
    OP(T_B, 1, 0, 0, 0),
    OP(T_H1, 1, 0, 0, 0),
    OP(T_H2, 1, 0, 0, 0),
    OP(T_Q, 1, 0, 0, 0),
    OP(T_A, 2, 0, 0, 1),
    OP(T_H1, 2, 0, 0, 1),
    OP(T_H2, 2, 0, 0, 1),
    OP(T_Q, 2, 0, 0, 0),
    OP(T_B, 3, 0, 1, 0),
    OP(T_Q, 3, 1, 1, 0),
    OP(T_A, 0, 1, 1, 0),
    OP(T_H1, 3, 0, 1, 0),
    OP(T_H1, 0, 1, 1, 0),
    OP(T_H2, 0, 1, 1, 0),
};
constexpr unsigned long long pack4(int i) { return prog[i] | (prog[i + 1] << 16) | (prog[i + 2] << 32) | (prog[i + 3] << 48); }

__global__ void __launch_bounds__(256, 1) __attribute__((amdgpu_num_vgpr(64))) mega_kernel(Params p) {
  extern __shared__ __attribute__((aligned(16))) char smem[];
  asm volatile("" ::: "v255", "a255");
  LAS char* L = (LAS char*)smem;
  const int w_ = __builtin_amdgcn_readfirstlane(threadIdx.x >> 6), bid_ = blockIdx.x;
  const int grp = (bid_ >> 3) & 1;
  const bool writer = (bid_ & 7) == 0;
  const rsrc_t rs = __builtin_amdgcn_make_buffer_rsrc((void*)(p.ws + (size_t)grp * W_GROUP), 0, W_GROUP * 4, 0x00020000);
#define IDS() \
  const int lane = lane_id(), w = fresh_s(w_), tid = w * 64 + lane, n16 = lane & 15, g4 = lane >> 4, bid = fresh_s(bid_), team = bid & 7, j = bid >> 4; \
  (void)tid; (void)lane; (void)n16; (void)g4; (void)w; (void)team; (void)j
#define STREAM_ARGS(R) \
  const int lk = (n16 >> 1) * 256 + (n16 & 1) * 64 + g4 * 16, lv = g4 * 2048 + n16 * 16; \
  const size_t srow = ((size_t)((grp + 2 * (R)) * 32 + 4 * team + (j >> 2)) * SK + 1024 * (j & 3) + 256 * w) * 128; \
  const v4u skB = make_srd(p.kB + srow, 65536), svB = make_srd(p.vB + srow, 65536)
#define TICK() do { \
    if (ndma != 0) { if (row < 3 && nq < 4 * (row + 1)) { IDS(); dma_a(p, grp + 2 * (row + 1), team, j, w, lane, nq & 3); ++nq; } } \
    else if (nburst != 0 && tok != 0u) { IDS(); STREAM_ARGS(strow); \
      for (int k_ = 0; k_ < nburst && nb < NHB; ++k_) { half_burst(nb, lk, lv, skB, svB); ++nb; } } } while (0)
#define SETTLE() __builtin_amdgcn_s_waitcnt(0x0f70)
#define SEND_TOK() do { if (w == 0 && row + grp < 1) { \
    const rsrc_t rso = __builtin_amdgcn_make_buffer_rsrc((void*)(p.ws + (size_t)(grp ^ 1) * W_GROUP), 0, W_GROUP * 4, 0x00020000); \
    st1(rso, (W_TOK + (team * 16 + j) * 16 + row + grp) * 4, 1u); } } while (0)
#define DMA_B2() do { if (!first && row < 3 && nq < 4 * (row + 1)) { IDS(); dma_a(p, grp + 2 * (row + 1), team, j, w, lane, nq & 3); ++nq; } } while (0)
#define CATCH_UP(N) do { while (nb < (N)) { half_burst(nb, lk, lv, skB, svB); ++nb; } } while (0)
#define TOKOFF ((W_TOK + (team * 16 + j) * 16 + strow) * 4)
#define POLL_TOK() do { if (tok == 0u && nburst != 0) tok = __builtin_amdgcn_readfirstlane(tok | ld1(rs, TOKOFF)); } while (0)

  int nb = 0;
  unsigned tok = grp == 0 ? 1u : 0u;
  {
    v4u wv[8];
    { IDS(); load_w(wv, p.wqA + 512 * team + 32 * j, QD, tid); }
    for (int r = 0; r < 4; ++r) {
      IDS();
      const unsigned xr = *(const unsigned*)(p.x + (grp + 2 * r) * DM + 2 * tid);
      dma_a(p, grp, team, j, w, lane, r);
      const v4u a = ln_stage(xr, L, tid, L_O);
      const float q = gemv32(wv, a, L, tid);
      if (tid < 32) st1(rs, (W_Q + (3 + r) * QD + 512 * team + 32 * j + tid) * 4, (f2bf(q) << 16) | (1u + (unsigned)r % 3u));
    }
  }
  unsigned cap = SPIN_CAP;
  int nq = 0;

  for (int pc = 11 - NOPS; pc < 4 * NOPS + 2; ++pc) {
    const int it = (pc >> 4), idx = pc & 15;
    const unsigned long long pw = (idx & 8) ? ((idx & 4) ? pack4(12) : pack4(8)) : ((idx & 4) ? pack4(4) : pack4(0));
    const unsigned op = (unsigned)(pw >> (16 * (idx & 3))) & 0xffffu;
    const int type = op & 7, kind = (op >> 3) & 3, row = it + (int)((op >> 5) & 3) - 1, ndma = (op >> 10) & 7;
    const int strow = idx >= PC0 ? it + 1 : it;
    const int nburst = strow < 4 ? (op >> 7) & 7 : 0;
    if (row < 0 || row > 3) continue;
    tok = __builtin_amdgcn_readfirstlane(tok); nb = __builtin_amdgcn_readfirstlane(nb); nq = __builtin_amdgcn_readfirstlane(nq); cap = __builtin_amdgcn_readfirstlane(cap);
    const unsigned tag = 1u + (unsigned)row % 3u, ptag = (tag & 1u) | ((tag & 2u) << 15);
    const bool isA = (kind & 1) == 0;

    if (type == T_Q) {
      IDS();
      const bool forB = (kind & 1) == 0;
      if (!forB || w == 0) {
        const int qoff = W_Q + (kind == 3 ? 3 + row : kind) * QD + 512 * team + (forB ? 128 * (j >> 2) : 0);
        unsigned spins = 0;
        v2u qv = ld2(rs, tid * 8, qoff * 4);
        for (;;) {
          cfence();
          const v2u qn = ld2(rs, tid * 8, qoff * 4);
          if (none((((qv.x ^ tag) | (qv.y ^ tag)) & 3u) != 0u) || ++spins > cap) { if (spins > cap) cap = 0u; break; }
          POLL_TOK();
          qv = qn;
        }
        *(LAS unsigned*)(L + L_Q + tid * 4) = (qv.x >> 16) | (qv.y & 0xffff0000u);
      }
      lds_barrier();
      SETTLE(); TICK();
    } else if (type == T_A || type == T_B) {
      if (type == T_A) {
        float sc[4];
        { IDS(); a_scores(sc[0], sc[1], sc[2], sc[3], L_AK + (64 * w + n16) * 256 + ((g4 ^ n16) << 4), L_Q + (n16 & 3) * 256 + g4 * 16); }
        if (kind == 0) TICK();
        IDS();
        LAS float* comb = (LAS float*)(L + L_U) + w * 4 * CREC;
        LAS u16* pex = (LAS u16*)comb;
        const int ppos = 32 * (g4 >> 1) + 8 * (n16 >> 2) + 4 * (g4 & 1) + (n16 & 3);
        float mh[4];
#pragma unroll
        for (int h = 0; h < 4; ++h) { sc[h] = rbf(rbf(sc[h]) * SCALE) * LOG2E; mh[h] = wave_max(sc[h]); }
#pragma unroll
        for (int h = 0; h < 4; ++h) pex[h * 64 + ppos] = (u16)f2bf(ex2(sc[h] - mh[h]));
        const v4u pb0 = *(const LAS v4u*)(pex + (n16 & 3) * 64 + 8 * g4), pb1 = *(const LAS v4u*)(pex + (n16 & 3) * 64 + 32 + 8 * g4);
        const float mm = lane == 0 ? mh[0] : lane == 1 ? mh[1] : lane == 2 ? mh[2] : mh[3];
        const int q_ = (lane >> 2) & 3, p_ = lane & 3;
        const float ll = a_pv(pb0, pb1, L_AV + (64 * w + 4 * g4 + q_) * 256 + ((((p_ >> 1) ^ (2 * (4 * (g4 & 1) + q_)))) << 4) + (p_ & 1) * 8,
                              L_U + (w * 4 * CREC + n16 * CREC + 4 * g4) * 4);
        { IDS(); if (lane < 4) *(LAS f32x2*)(L + L_U + ((w * 4 + lane) * CREC + 128) * 4) = f32x2{mm, ll}; }
      } else {
        const bool first = kind == 1;
        if (first) {
          {
            IDS(); STREAM_ARGS(row);
            unsigned spins = 0;
            while (tok == 0u && ++spins < cap) { cfence(); tok = __builtin_amdgcn_readfirstlane(tok | ld1(rs, TOKOFF)); __builtin_amdgcn_s_sleep(2); }
            CATCH_UP(14);
          }
        }
        v4u qa0 = {0u, 0u, 0u, 0u}, qa1 = {0u, 0u, 0u, 0u}, msk;
        {
          IDS();
          if ((n16 & 2) == 0) {
            qa0 = *(const LAS v4u*)(L + L_Q + (4 * (n16 & 1) + g4) * 16);
            qa1 = *(const LAS v4u*)(L + L_Q + (8 + 4 * (n16 & 1) + g4) * 16);
          }
          const unsigned hm = (n16 & 1) ? 0xffff0000u : 0x0000ffffu;
          const int ri = n16 >> 1;
          msk.x = ri == 0 ? hm : 0u; msk.y = ri == 1 ? hm : 0u; msk.z = ri == 2 ? hm : 0u; msk.w = ri == 3 ? hm : 0u;
          if (first) wait_vm<63>();
        }
        BState st; st.acc = f32x4{0.f, 0.f, 0.f, 0.f}; st.m = -INFINITY; st.l = 0.f;
        b_stage_last(st, qa0, qa1, msk);
        {
          IDS(); STREAM_ARGS(row);
          const int sv_off = L_RB + w * (NSAVE * 1024) + lane * 16;
          if (first) {
            asm volatile("ds_write_b128 %0, v[236:239]\n\tds_write_b128 %0, v[240:243] offset:1024\n\tds_write_b128 %0, v[244:247] offset:2048\n\t"
                         "ds_write_b128 %0, v[248:251] offset:3072\n\tds_write_b128 %0, v[252:255] offset:4096\n\ts_waitcnt lgkmcnt(0)" ::"v"(sv_off) : "memory");
            ld_unit<6, 0, 16>(lk, lv, skB, svB, 7);
            wait_vm<63>();
          } else {
            ld_unit<6, 0, 16 - NSAVE>(lk, lv, skB, svB, 0);
            asm volatile("ds_read_b128 v[236:239], %0\n\tds_read_b128 v[240:243], %0 offset:1024\n\tds_read_b128 v[244:247], %0 offset:2048\n\t"
                         "ds_read_b128 v[248:251], %0 offset:3072\n\tds_read_b128 v[252:255], %0 offset:4096" ::"v"(sv_off) : "memory");
          }
        }
        const int nq0 = nq;
        DMA_B2(); DMA_B2();
        b_stage_012(st, qa0, qa1, msk);
        if (first) wait_vm<16>();
        DMA_B2();
        b_stage_345(st, qa0, qa1, msk);
        if (nq - nq0 == 3) wait_vm<24>(); else wait_vm<0>();
        asm volatile("s_waitcnt lgkmcnt(0)" ::: "memory");
        DMA_B2();
        if (first) { IDS(); SEND_TOK(); }
        b_stage_last(st, qa0, qa1, msk);
        {
          IDS();
          LAS float* comb = (LAS float*)(L + L_U) + w * 4 * CREC;
          const float lsum = wave_sum(st.l) * 0.5f;
          if (n16 < 8) {
            comb[32 * g4 + n16] = st.acc.x; comb[32 * g4 + 8 + n16] = st.acc.y; comb[32 * g4 + 16 + n16] = st.acc.z; comb[32 * g4 + 24 + n16] = st.acc.w;
          }
          if (lane == 0) *(LAS f32x2*)(comb + 128) = f32x2{st.m, lsum};
        }
        if (!first) nb = 0;
      }
      lds_barrier();
      {
        IDS();
        const bool isml = tid >= 128;
        const int h = isml ? tid - 128 : isA ? tid >> 4 : 0;
        if (isml ? h < (isA ? 4 : 1) : tid < (isA ? 64 : 32)) {
          const LAS float* cb = (const LAS float*)(L + L_U) + h * CREC;
          const f32x2 a0 = *(const LAS f32x2*)(cb + 128), a1 = *(const LAS f32x2*)(cb + 4 * CREC + 128), a2 = *(const LAS f32x2*)(cb + 8 * CREC + 128),
                      a3 = *(const LAS f32x2*)(cb + 12 * CREC + 128);
          const float M = fmaxf(fmaxf(a0.x, a1.x), fmaxf(a2.x, a3.x));
          const float f0 = ex2(a0.x - M), f1 = ex2(a1.x - M), f2 = ex2(a2.x - M), f3 = ex2(a3.x - M);
          const int rec = isA ? W_P1A + (kind >> 1) * SZ_P1A + ((team * 16 + j) * 4 + h) * RECA : W_P1B + (kind >> 1) * SZ_P1B + (team * 16 + j) * RECB;
          if (isml) {
            st2(rs, (rec + (isA ? 64 : 128)) * 4, v2u{tagw(M, tag), tagw((f0 * a0.y + f1 * a1.y) + (f2 * a2.y + f3 * a3.y), tag)});
          } else if (isA) {
            const int d = 8 * (tid & 15);
            const f32x4 o = (*(const LAS f32x4*)(cb + d) * f0 + *(const LAS f32x4*)(cb + 4 * CREC + d) * f1) +
                            (*(const LAS f32x4*)(cb + 8 * CREC + d) * f2 + *(const LAS f32x4*)(cb + 12 * CREC + d) * f3);
            const f32x4 q = (*(const LAS f32x4*)(cb + d + 4) * f0 + *(const LAS f32x4*)(cb + 4 * CREC + d + 4) * f1) +
                            (*(const LAS f32x4*)(cb + 8 * CREC + d + 4) * f2 + *(const LAS f32x4*)(cb + 12 * CREC + d + 4) * f3);
            st4(rs, (rec + 4 * (tid & 15)) * 4, v4u{tagp(o.x, o.y, ptag), tagp(o.z, o.w, ptag), tagp(q.x, q.y, ptag), tagp(q.z, q.w, ptag)});
          } else {
            const int d = 4 * tid;
            const f32x4 o = (*(const LAS f32x4*)(cb + d) * f0 + *(const LAS f32x4*)(cb + 4 * CREC + d) * f1) +
                            (*(const LAS f32x4*)(cb + 8 * CREC + d) * f2 + *(const LAS f32x4*)(cb + 12 * CREC + d) * f3);
            st4(rs, (rec + d) * 4, v4u{tagw(o.x, tag), tagw(o.y, tag), tagw(o.z, tag), tagw(o.w, tag)});
          }
        }
      }
      TICK();
    } else if (type == T_H1) {
      v4u wv[8];
      unsigned pr;
      { IDS(); load_w<0, 6>(wv, (isA ? p.woA : p.woB) + (size_t)512 * team * DM + 32 * j, DM, tid); }
      {
        IDS();
        unsigned spins = 0;
        float o0 = 0.f, o1 = 0.f;
        if (isA) {
          const int base = (W_P1A + (kind >> 1) * SZ_P1A + (team * 64 + w) * RECA) * 4;
          v2u ml; unsigned o[16];
          for (;;) {
            cfence();
            ml = ld2(rs, n16 * (4 * RECA * 4) + 256, base);
#pragma unroll
            for (int s = 0; s < 16; ++s) o[s] = ld1(rs, lane * 4, base + s * (4 * RECA * 4));
            POLL_TOK();
            unsigned aa = o[0], oo = o[0];
#pragma unroll
            for (int s = 1; s < 16; ++s) { aa &= o[s]; oo |= o[s]; }
            const unsigned bad = (((aa ^ ptag) | (oo ^ ptag)) & 0x10001u) | (((ml.x ^ tag) | (ml.y ^ tag)) & 3u);
            if (none(bad != 0u) || ++spins > cap) { if (spins > cap) cap = 0u; break; }
          }
          cfence();
          load_w<6, 8>(wv, p.woA + (size_t)512 * team * DM + 32 * j, DM, tid);
          const float m = asf(ml.x), M = row_max(m), wt = ex2(m - M);
          const float f = wt * __builtin_amdgcn_rcpf(row_sum(wt * asf(ml.y)));
#pragma unroll
          for (int s = 0; s < 16; ++s) { const float fs = rdl(f, s); o0 = fmaf(fs, lo(o[s]), o0); o1 = fmaf(fs, hi(o[s]), o1); }
        } else {
          const int base = (W_P1B + (kind >> 1) * SZ_P1B + (team * 16 + 4 * w) * RECB) * 4;
          load_w<6, 8>(wv, p.woB + (size_t)512 * team * DM + 32 * j, DM, tid);
          v2u ml, o[4];
          for (;;) {
            cfence();
            ml = ld2(rs, (lane & 3) * (RECB * 4) + 512, base);
#pragma unroll
            for (int s = 0; s < 4; ++s) o[s] = ld2(rs, lane * 8, base + s * (RECB * 4));
            POLL_TOK();
            unsigned aa = ml.x & ml.y, oo = ml.x | ml.y;
#pragma unroll
            for (int s = 0; s < 4; ++s) { aa &= o[s].x & o[s].y; oo |= o[s].x | o[s].y; }
            if (none((((aa ^ tag) | (oo ^ tag)) & 3u) != 0u) || ++spins > cap) { if (spins > cap) cap = 0u; break; }
          }
          float m = asf(ml.x), M = fmaxf(m, dppf<0xB1>(m)); M = fmaxf(M, dppf<0x4E>(M));
          const float wt = ex2(m - M);
          float ls = wt * asf(ml.y); ls += dppf<0xB1>(ls); ls += dppf<0x4E>(ls);
          const float f = wt * __builtin_amdgcn_rcpf(ls);
#pragma unroll
          for (int s = 0; s < 4; ++s) { const float fs = rdl(f, s); o0 = fmaf(fs, asf(o[s].x), o0); o1 = fmaf(fs, asf(o[s].y), o1); }
        }
        pr = f2bf(o0) | (f2bf(o1) << 16);
      }
      { IDS(); *(LAS unsigned*)(L + L_O + tid * 4) = pr; }
      SETTLE(); TICK();
      IDS();
      lds_barrier();
      const float y = gemv32(wv, *(const LAS v4u*)(L + L_O + (tid >> 2) * 16), L, tid);
      if (tid < 32) st1(rs, (W_Y + (kind * 8 + team) * DM + 32 * j + tid) * 4, tagw(y, tag));
      TICK();
    } else if (kind == 3) {
      if (writer && w_ == 0) {
        IDS();
        unsigned yv[8], spins = 0;
        for (;;) {
          cfence();
#pragma unroll
          for (int k = 0; k < 8; ++k) yv[k] = ld1(rs, (lane & 31) * 4, (W_Y + (3 * 8 + k) * DM + 32 * j) * 4);
          unsigned aa = yv[0], oo = yv[0];
#pragma unroll
          for (int k = 1; k < 8; ++k) { aa &= yv[k]; oo |= yv[k]; }
          if (none((((aa ^ tag) | (oo ^ tag)) & 3u) != 0u) || ++spins > cap) { if (spins > cap) cap = 0u; break; }
        }
        const float ys = ((asf(yv[0]) + asf(yv[1])) + (asf(yv[2]) + asf(yv[3]))) + ((asf(yv[4]) + asf(yv[5])) + (asf(yv[6]) + asf(yv[7])));
        if (lane < 32) p.out[(grp + 2 * row) * DM + 32 * j + lane] = (u16)f2bf(bf2f(*(const LAS u16*)(L + L_XS + lane * 2)) + rbf(ys));
      }
    } else {
      v4u wv[8];
      unsigned xo;
      {
        IDS();
        load_w(wv, (isA ? p.wqB : p.wqA) + 512 * team + 32 * j, QD, tid);
        xo = kind == 0 ? *(const unsigned*)(p.x + (grp + 2 * row) * DM + 2 * tid) : *(const LAS unsigned*)(L + L_XP + tid * 4);
      }
      IDS();
      v2u yv[8];
      unsigned spins = 0;
      for (;;) {
        cfence();
#pragma unroll
        for (int k = 0; k < 8; ++k) yv[k] = ld2(rs, tid * 8, (W_Y + (kind * 8 + k) * DM) * 4);
        POLL_TOK();
        unsigned aa = ~0u, oo = 0u;
#pragma unroll
        for (int k = 0; k < 8; ++k) { aa &= yv[k].x & yv[k].y; oo |= yv[k].x | yv[k].y; }
        if (none((((aa ^ tag) | (oo ^ tag)) & 3u) != 0u) || ++spins > cap) { if (spins > cap) cap = 0u; break; }
      }
      const float y0 = ((asf(yv[0].x) + asf(yv[1].x)) + (asf(yv[2].x) + asf(yv[3].x))) + ((asf(yv[4].x) + asf(yv[5].x)) + (asf(yv[6].x) + asf(yv[7].x)));
      const float y1 = ((asf(yv[0].y) + asf(yv[1].y)) + (asf(yv[2].y) + asf(yv[3].y))) + ((asf(yv[4].y) + asf(yv[5].y)) + (asf(yv[6].y) + asf(yv[7].y)));
      const unsigned xn = f2bf(lo(xo) + rbf(y0)) | (f2bf(hi(xo) + rbf(y1)) << 16);
      if (kind == 2 && (tid >> 4) == j) *(LAS unsigned*)(L + L_XS + (tid & 15) * 4) = xn;
      SETTLE(); TICK();
      const v4u a = ln_stage(xn, L, tid, L_XP);
      const float q = gemv32(wv, a, L, tid);
      if (tid < 32) st1(rs, (W_Q + kind * QD + 512 * team + 32 * j + tid) * 4, (f2bf(q) << 16) | tag);
      TICK();
    }
  }
}
}

extern "C" void kernel_launch(void* const* d_in, const int* in_sizes, int n_in, void* d_out, int out_size, void* d_ws, size_t ws_size, hipStream_t stream) {
  typedef const mega::u16* cp;
  mega::Params p;
  p.x = (cp)d_in[0]; p.kA = (cp)d_in[1]; p.vA = (cp)d_in[2]; p.wqA = (cp)d_in[3]; p.woA = (cp)d_in[4];
  p.kB = (cp)d_in[6]; p.vB = (cp)d_in[7]; p.wqB = (cp)d_in[8]; p.woB = (cp)d_in[9];
  p.out = (mega::u16*)d_out; p.ws = (unsigned*)d_ws;
  (void)hipMemsetAsync(d_ws, 0, mega::WS_BYTES, stream);
  (void)hipFuncSetAttribute((const void*)mega::mega_kernel, hipFuncAttributeMaxDynamicSharedMemorySize, 163840);
  mega::mega_kernel<<<256, 256, 163840, stream>>>(p);
}
```

```cpp
#include <hip/hip_runtime.h>
#include <hip/hip_bf16.h>
#include <stdint.h>

namespace mega {
typedef unsigned short u16;
typedef unsigned v4u __attribute__((ext_vector_type(4)));
typedef unsigned v2u __attribute__((ext_vector_type(2)));
typedef float f32x4 __attribute__((ext_vector_type(4)));
typedef float f32x2 __attribute__((ext_vector_type(2)));
#define LAS __attribute__((address_space(3)))
#define DEV __device__ __forceinline__

constexpr int DM = 512, SK = 4096, QD = 4096;
constexpr float SCALE = 0.08838834764831845f, LOG2E = 1.4426950408889634f;
constexpr unsigned SPIN_CAP = 1u << 17;

constexpr int RECA = 68;
constexpr int RECB = 132;
constexpr int SZ_P1A = 8 * 16 * 4 * RECA, SZ_P1B = 8 * 16 * RECB;
constexpr int W_P1A = 0, W_P1B = W_P1A + 2 * SZ_P1A, W_Y = W_P1B + 2 * SZ_P1B, W_Q = W_Y + 4 * 8 * DM,
              W_TOK = W_Q + 7 * QD, W_GROUP = W_TOK + 128 * 16;
constexpr size_t WS_BYTES = (size_t)2 * W_GROUP * 4;

constexpr int CREC = 132;
constexpr int NSAVE = 5;
constexpr int L_AK = 0, L_AV = 65536, L_RB = 131072, L_U = L_RB + 4 * NSAVE * 1024, L_O = L_U + 4 * 4 * CREC * 4,
              L_Q = L_O + 1024, L_XP = L_Q + 1024, L_ST = L_XP + 1024, L_XS = L_ST + 64, L_END = L_XS + 64;
constexpr int L_RED = L_U;
static_assert(L_END <= 163840, "LDS");

struct Params { const u16 *x, *kA, *vA, *wqA, *woA, *kB, *vB, *wqB, *woB; u16* out; unsigned* ws; };

#define ACLOB \
  "a0", "a1", "a2", "a3", "a4", "a5", "a6", "a7", "a8", "a9", "a10", "a11", "a12", "a13", "a14", "a15", \
  "a16", "a17", "a18", "a19", "a20", "a21", "a22", "a23", "a24", "a25", "a26", "a27", "a28", "a29", "a30", "a31", \
  "a32", "a33", "a34", "a35", "a36", "a37", "a38", "a39", "a40", "a41", "a42", "a43", "a44", "a45", "a46", "a47", \
  "a48", "a49", "a50", "a51", "a52", "a53", "a54", "a55", "a56", "a57", "a58", "a59", "a60", "a61", "a62", "a63"
#define V8(a, b, c, d, e, f, g, h) "v" #a, "v" #b, "v" #c, "v" #d, "v" #e, "v" #f, "v" #g, "v" #h
#define VCLOB_32_63 V8(32, 33, 34, 35, 36, 37, 38, 39), V8(40, 41, 42, 43, 44, 45, 46, 47), V8(48, 49, 50, 51, 52, 53, 54, 55), V8(56, 57, 58, 59, 60, 61, 62, 63)
#define VCLOB_8_31 V8(8, 9, 10, 11, 12, 13, 14, 15), V8(16, 17, 18, 19, 20, 21, 22, 23), V8(24, 25, 26, 27, 28, 29, 30, 31)
#define VCLOB_16_31 V8(16, 17, 18, 19, 20, 21, 22, 23), V8(24, 25, 26, 27, 28, 29, 30, 31)
#define SCLOB "s84", "s85", "s86", "s87", "s88", "s89", "s90", "s91", "s92", "s93", "s94", "s95"

DEV float bf2f(unsigned u) { return __builtin_bit_cast(float, u << 16); }
DEV unsigned f2bf(float f) { return (unsigned)__builtin_bit_cast(u16, (__bf16)f); }
DEV float rbf(float f) { return bf2f(f2bf(f)); }
DEV float lo(unsigned u) { return __builtin_bit_cast(float, u << 16); }
DEV float hi(unsigned u) { return __builtin_bit_cast(float, u & 0xffff0000u); }
DEV float asf(unsigned u) { return __builtin_bit_cast(float, u); }
DEV unsigned asu(float f) { return __builtin_bit_cast(unsigned, f); }
DEV unsigned tagw(float f, unsigned tag) { return (asu(f) & ~3u) | tag; }
DEV unsigned tagp(float a, float b, unsigned ptag) {
  return (((unsigned)(int)__builtin_rintf(a) << 1) & 0xffffu) | ((unsigned)(int)__builtin_rintf(b) << 17) | ptag;
}
DEV bool none(bool bad) { return __builtin_amdgcn_ballot_w64(bad) == 0ull; }
DEV float ex2(float x) { return __builtin_amdgcn_exp2f(x); }
template <int CTRL> DEV float dppf(float x) { return asf(__builtin_amdgcn_update_dpp(0u, asu(x), CTRL, 0xf, 0xf, true)); }
DEV float rdl(float x, int l) { return asf(__builtin_amdgcn_readlane(asu(x), l)); }
DEV float row_max(float x) {
  x = fmaxf(x, dppf<0xB1>(x)); x = fmaxf(x, dppf<0x4E>(x)); x = fmaxf(x, dppf<0x141>(x)); x = fmaxf(x, dppf<0x140>(x)); return x;
}
DEV float row_sum(float x) {
  x += dppf<0xB1>(x); x += dppf<0x4E>(x); x += dppf<0x141>(x); x += dppf<0x140>(x); return x;
}
template <int CTRL, int RM> DEV float dppk(float x) { return asf(__builtin_amdgcn_update_dpp(asu(x), asu(x), CTRL, RM, 0xf, false)); }
DEV float wave_max(float x) {
  x = row_max(x); x = fmaxf(x, dppk<0x142, 0xa>(x)); x = fmaxf(x, dppk<0x143, 0xc>(x)); return rdl(x, 63);
}
DEV float wave_sum(float x) {
  x = row_sum(x);
  x += asf(__builtin_amdgcn_update_dpp(0u, asu(x), 0x142, 0xa, 0xf, false));
  x += asf(__builtin_amdgcn_update_dpp(0u, asu(x), 0x143, 0xc, 0xf, false));
  return rdl(x, 63);
}
DEV void lds_barrier() { asm volatile("s_waitcnt lgkmcnt(0)\n\ts_barrier" ::: "memory", ACLOB); }
DEV void cfence() { asm volatile("" ::: "memory", ACLOB); }
DEV int lane_id() {
  int l;
  asm volatile("v_mbcnt_lo_u32_b32 %0, -1, 0\n\tv_mbcnt_hi_u32_b32 %0, -1, %0" : "=v"(l)::ACLOB);
  return l;
}
DEV int fresh_s(int x) { asm volatile("" : "+s"(x)); return x; }
template <int N> DEV void wait_vm() { asm volatile("s_waitcnt vmcnt(%c0)" ::"i"(N) : "memory"); }
DEV v4u make_srd(const void* p, unsigned bytes) {
  v4u s; s.x = (unsigned)(size_t)p; s.y = (unsigned)((size_t)p >> 32) & 0xffffu; s.z = bytes; s.w = 0x00020000u; return s;
}
typedef __amdgpu_buffer_rsrc_t rsrc_t;
DEV unsigned ld1(rsrc_t r, int off, int soff = 0) { return __builtin_amdgcn_raw_buffer_load_b32(r, off, soff, 16); }
DEV v2u ld2(rsrc_t r, int off, int soff = 0) { return __builtin_amdgcn_raw_buffer_load_b64(r, off, soff, 16); }
DEV void st1(rsrc_t r, int off, unsigned v) { __builtin_amdgcn_raw_buffer_store_b32(v, r, off, 0, 16); }
DEV void st2(rsrc_t r, int off, v2u v) { __builtin_amdgcn_raw_buffer_store_b64(v, r, off, 0, 16); }
DEV void st4(rsrc_t r, int off, v4u v) { __builtin_amdgcn_raw_buffer_store_b128(v, r, off, 0, 16); }

template <int F, int IMM> DEV void ld_frag(int voff, v4u srd, int soff) {
  if constexpr (F < 64)
    asm volatile("buffer_load_dwordx4 a[%c0:%c0+3], %1, %2, %3 offen offset:%c4 nt" ::"i"(4 * F), "v"(voff), "s"(srd), "s"(soff), "i"(IMM) : "memory");
  else
    asm volatile("buffer_load_dwordx4 v[%c0:%c0+3], %1, %2, %3 offen offset:%c4 nt" ::"i"(64 + 4 * (F - 64)), "v"(voff), "s"(srd), "s"(soff), "i"(IMM) : "memory");
}
template <int U, int I, int HI> DEV void ld_unit_from(int lk, int lv, v4u sk, v4u sv, int s0, int s1) {
  if constexpr (I < HI) {
    if constexpr (I < 8) ld_frag<16 * U + I, ((I >> 1) & 1) * 2048 + (I & 1) * 128>(lk, sk, (I >> 2) ? s1 : s0);
    else ld_frag<16 * U + I, (I - 8) * 256>(lv, sv, s0);
    ld_unit_from<U, I + 1, HI>(lk, lv, sk, sv, s0, s1);
  }
}
template <int U, int LO, int HI> DEV void ld_unit(int lk, int lv, v4u sk, v4u sv, int logical) {
  const int s0 = logical * 8192, s1 = s0 + 4096;
  asm volatile("s_nop 4" ::: "memory");
  ld_unit_from<U, LO, HI>(lk, lv, sk, sv, s0, s1);
}
DEV void half_burst(int h, int lk, int lv, v4u sk, v4u sv) {
  switch (h) {
    case 0: ld_unit<6, 0, 8>(lk, lv, sk, sv, 0); break;
    case 1: ld_unit<6, 8, 16>(lk, lv, sk, sv, 0); break;
    case 2: ld_unit<0, 0, 8>(lk, lv, sk, sv, 1); break;
    case 3: ld_unit<0, 8, 16>(lk, lv, sk, sv, 1); break;
    case 4: ld_unit<1, 0, 8>(lk, lv, sk, sv, 2); break;
    case 5: ld_unit<1, 8, 16>(lk, lv, sk, sv, 2); break;
    case 6: ld_unit<2, 0, 8>(lk, lv, sk, sv, 3); break;
    case 7: ld_unit<2, 8, 16>(lk, lv, sk, sv, 3); break;
    case 8: ld_unit<3, 0, 8>(lk, lv, sk, sv, 4); break;
    case 9: ld_unit<3, 8, 16>(lk, lv, sk, sv, 4); break;
    case 10: ld_unit<4, 0, 8>(lk, lv, sk, sv, 5); break;
    case 11: ld_unit<4, 8, 16>(lk, lv, sk, sv, 5); break;
    case 12: ld_unit<5, 0, 8>(lk, lv, sk, sv, 6); break;
    case 13: ld_unit<5, 8, 16>(lk, lv, sk, sv, 6); break;
    default: break;
  }
}
constexpr int NHB = 14;

#define MF "v_mfma_f32_16x16x32_bf16 "
#define B_SETUP \
  "s_mov_b32 s84, 0xffff0000\n\ts_mov_b32 s85, 0\n\ts_mov_b32 s86, 0\n\ts_mov_b32 s87, 0x0000ffff\n\t" \
  "s_mov_b32 s88, 0\n\ts_mov_b32 s89, 0xffff0000\n\ts_mov_b32 s90, 0xaaaaaaaa\n\ts_mov_b32 s91, 0xaaaaaaaa\n\ts_nop 1\n\t"
#define B_QK(P, R, BK) \
  MF "v[" #BK ":" #BK "+3], %[qa0], " P "[%c[" R "]:%c[" R "]+3], 0\n\t" \
  MF "v[" #BK "+4:" #BK "+7], %[qa0], " P "[%c[" R "]+8:%c[" R "]+11], 0\n\t" \
  MF "v[" #BK "+8:" #BK "+11], %[qa0], " P "[%c[" R "]+16:%c[" R "]+19], 0\n\t" \
  MF "v[" #BK "+12:" #BK "+15], %[qa0], " P "[%c[" R "]+24:%c[" R "]+27], 0\n\t" \
  MF "v[" #BK ":" #BK "+3], %[qa1], " P "[%c[" R "]+4:%c[" R "]+7], v[" #BK ":" #BK "+3]\n\t" \
  MF "v[" #BK "+4:" #BK "+7], %[qa1], " P "[%c[" R "]+12:%c[" R "]+15], v[" #BK "+4:" #BK "+7]\n\t" \
  MF "v[" #BK "+8:" #BK "+11], %[qa1], " P "[%c[" R "]+20:%c[" R "]+23], v[" #BK "+8:" #BK "+11]\n\t" \
  MF "v[" #BK "+12:" #BK "+15], %[qa1], " P "[%c[" R "]+28:%c[" R "]+31], v[" #BK "+12:" #BK "+15]\n\t"
#define B_MERGE(BK, T) \
  "v_cndmask_b32_e64 v[" #BK "+2], v[" #BK "], v[" #BK "+4], s[84:85]\n\t" \
  "v_cndmask_b32_e64 v[" #BK "+3], v[" #BK "+1], v[" #BK "+5], s[84:85]\n\t" \
  "v_cndmask_b32_e64 v[" #BK "+2], v[" #BK "+2], v[" #BK "+8], s[86:87]\n\t" \
  "v_cndmask_b32_e64 v[" #BK "+3], v[" #BK "+3], v[" #BK "+9], s[86:87]\n\t" \
  "v_cndmask_b32_e64 v[" #BK "+2], v[" #BK "+2], v[" #BK "+12], s[88:89]\n\t" \
  "v_cndmask_b32_e64 v[" #BK "+3], v[" #BK "+3], v[" #BK "+13], s[88:89]\n\t" \
  "v_cndmask_b32_e64 v[" #BK "+2], v[" #BK "+2], v[" #BK "+3], s[90:91]\n\t" \
  "s_nop 1\n\t" \
  "v_add_f32_dpp v[" #BK "+3], v[" #BK "+2], v[" #BK "+2] quad_perm:[1,0,3,2] row_mask:0xf bank_mask:0xf\n\t" \
  "v_cvt_pk_bf16_f32 v[" #BK "+3], v[" #BK "+3], v[" #BK "+3]\n\t" \
  "v_and_b32 v[" #BK "+3], 0xffff0000, v[" #BK "+3]\n\t" \
  "v_mul_f32 v[" #BK "+3], 0x3db504f3, v[" #BK "+3]\n\t" \
  "v_cvt_pk_bf16_f32 v[" #BK "+3], v[" #BK "+3], v[" #BK "+3]\n\t" \
  "v_and_b32 v[" #BK "+3], 0xffff0000, v[" #BK "+3]\n\t" \
  "v_mul_f32 " T ", 0x3fb8aa3b, v[" #BK "+3]\n\t"
#define B_MAX \
  "s_nop 1\n\tv_max_f32_dpp v27, v27, v27 quad_perm:[1,0,3,2] row_mask:0xf bank_mask:0xf\n\t" \
  "s_nop 1\n\tv_max_f32_dpp v27, v27, v27 quad_perm:[2,3,0,1] row_mask:0xf bank_mask:0xf\n\t" \
  "s_nop 1\n\tv_max_f32_dpp v27, v27, v27 row_half_mirror row_mask:0xf bank_mask:0xf\n\t" \
  "s_nop 1\n\tv_max_f32_dpp v27, v27, v27 row_mirror row_mask:0xf bank_mask:0xf\n\t" \
  "s_nop 1\n\t" \
  "v_readlane_b32 s92, v27, 0\n\tv_readlane_b32 s93, v27, 16\n\tv_readlane_b32 s94, v27, 32\n\tv_readlane_b32 s95, v27, 48\n\t" \
  "v_max_f32 v27, s92, %[m]\n\tv_max_f32 v27, s93, v27\n\ts_nop 0\n\tv_max_f32 v27, s94, v27\n\tv_max_f32 v27, s95, v27\n\t" \
  "v_sub_f32 v32, %[m], v27\n\tv_exp_f32 v32, v32\n\tv_mov_b32 %[m], v27\n\t" \
  "v_mul_f32 v28, v28, v32\n\tv_mul_f32 v29, v29, v32\n\tv_mul_f32 v30, v30, v32\n\tv_mul_f32 v31, v31, v32\n\tv_mul_f32 %[l], %[l], v32\n\t"
#define B_EXP(T) "v_sub_f32 " T ", " T ", v27\n\tv_exp_f32 " T ", " T "\n\t"
#define B_ACC(T) "v_add_f32 %[l], %[l], " T "\n\tv_cvt_pk_bf16_f32 " T ", " T ", " T "\n\t"
#define B_AND(S, T, LN) \
  "v_and_b32_dpp v[" #S "], " T ", %[k0] row_newbcast:" #LN " row_mask:0xf bank_mask:0xf\n\t" \
  "v_and_b32_dpp v[" #S "+1], " T ", %[k1] row_newbcast:" #LN " row_mask:0xf bank_mask:0xf\n\t" \
  "v_and_b32_dpp v[" #S "+2], " T ", %[k2] row_newbcast:" #LN " row_mask:0xf bank_mask:0xf\n\t" \
  "v_and_b32_dpp v[" #S "+3], " T ", %[k3] row_newbcast:" #LN " row_mask:0xf bank_mask:0xf\n\t"
#define B_PVM(P, R, O, S) MF "v[28:31], " P "[%c[" R "]+" #O ":%c[" R "]+" #O "+3], v[" #S ":" #S "+3], v[28:31]\n\t"
#define B_PV(P, R, T) \
  B_AND(32, T, 0) B_AND(36, T, 2) B_PVM(P, R, 32, 32) B_AND(40, T, 4) B_PVM(P, R, 36, 36) B_AND(32, T, 6) B_PVM(P, R, 40, 40) \
  B_AND(36, T, 8) B_PVM(P, R, 44, 32) B_AND(40, T, 10) B_PVM(P, R, 48, 36) B_AND(32, T, 12) B_PVM(P, R, 52, 40) \
  B_AND(36, T, 14) B_PVM(P, R, 56, 32) "s_nop 1\n\t" B_PVM(P, R, 60, 36)

struct BState { f32x4 acc; float m, l; };
#define B_OPERANDS(...) \
  : "+{v[28:31]}"(st.acc), [m] "+v"(st.m), [l] "+v"(st.l) \
  : [qa0] "v"(qa0), [qa1] "v"(qa1), [k0] "v"(msk.x), [k1] "v"(msk.y), [k2] "v"(msk.z), [k3] "v"(msk.w), __VA_ARGS__ \
  : "v24", "v25", "v26", "v27", VCLOB_32_63, SCLOB, ACLOB, "memory"
DEV void b_stage_last(BState& st, v4u qa0, v4u qa1, v4u msk) {
  asm volatile(B_SETUP B_QK("v", "r0", 32) "s_nop 7\n\t" B_MERGE(32, "v24") "v_mov_b32 v27, v24\n\t" B_MAX B_EXP("v24") "s_nop 0\n\t" B_ACC("v24") "s_nop 1\n\t"
               B_PV("v", "r0", "v24") "s_nop 7"
               B_OPERANDS([r0] "i"(192)));
}
#define B_STAGE3(P0, P1, P2) \
  B_SETUP B_QK(P0, "r0", 32) B_QK(P1, "r1", 48) B_MERGE(32, "v24") B_QK(P2, "r2", 32) B_MERGE(48, "v25") "s_nop 3\n\t" B_MERGE(32, "v26") \
  "v_max3_f32 v27, v24, v25, v26\n\t" B_MAX B_EXP("v24") B_EXP("v25") B_EXP("v26") "s_nop 0\n\t" B_ACC("v24") B_ACC("v25") B_ACC("v26") "s_nop 1\n\t" \
  B_PV(P0, "r0", "v24") B_PV(P1, "r1", "v25") B_PV(P2, "r2", "v26") "s_nop 7"
DEV void b_stage_012(BState& st, v4u qa0, v4u qa1, v4u msk) {
  asm volatile(B_STAGE3("a", "a", "a") B_OPERANDS([r0] "i"(0), [r1] "i"(64), [r2] "i"(128)));
}
DEV void b_stage_345(BState& st, v4u qa0, v4u qa1, v4u msk) {
  asm volatile(B_STAGE3("a", "v", "v") B_OPERANDS([r0] "i"(192), [r1] "i"(64), [r2] "i"(128)));
}

#define A_RD(BK, XOR, S16) \
  "v_xor_b32 %[t], " #XOR ", %[kb]\n\t" \
  "ds_read_b128 v[" #BK ":" #BK "+3], %[qb] offset:" #S16 "\n\t" \
  "ds_read_b128 v[" #BK "+4:" #BK "+7], %[t]\n\t" \
  "ds_read_b128 v[" #BK "+8:" #BK "+11], %[t] offset:4096\n\t" \
  "ds_read_b128 v[" #BK "+12:" #BK "+15], %[t] offset:8192\n\t" \
  "ds_read_b128 v[" #BK "+16:" #BK "+19], %[t] offset:12288\n\t"
#define A_MM(BK, C0, C1, C2, C3) \
  MF "v[8:11], v[" #BK ":" #BK "+3], v[" #BK "+4:" #BK "+7], " C0 "\n\t" \
  MF "v[12:15], v[" #BK ":" #BK "+3], v[" #BK "+8:" #BK "+11], " C1 "\n\t" \
  MF "v[16:19], v[" #BK ":" #BK "+3], v[" #BK "+12:" #BK "+15], " C2 "\n\t" \
  MF "v[20:23], v[" #BK ":" #BK "+3], v[" #BK "+16:" #BK "+19], " C3 "\n\t"
#define A_SEL(O, H) \
  "v_cndmask_b32_e64 " O ", v[8+" #H "], v[12+" #H "], s[84:85]\n\t" \
  "v_cndmask_b32_e64 " O ", " O ", v[16+" #H "], s[86:87]\n\t" \
  "v_cndmask_b32_e64 " O ", " O ", v[20+" #H "], s[88:89]\n\t"
DEV void a_scores(float& s0, float& s1, float& s2, float& s3, int kb, int qb) {
  int t;
  asm volatile(
      "s_waitcnt lgkmcnt(0)\n\t" B_SETUP
      A_RD(24, 0, 0) A_RD(44, 64, 64) "s_waitcnt lgkmcnt(5)\n\t" A_MM(24, "0", "0", "0", "0")
      A_RD(24, 128, 128) "s_waitcnt lgkmcnt(5)\n\t" A_MM(44, "v[8:11]", "v[12:15]", "v[16:19]", "v[20:23]")
      A_RD(44, 192, 192) "s_waitcnt lgkmcnt(5)\n\t" A_MM(24, "v[8:11]", "v[12:15]", "v[16:19]", "v[20:23]")
      "s_waitcnt lgkmcnt(0)\n\t" A_MM(44, "v[8:11]", "v[12:15]", "v[16:19]", "v[20:23]")
      "s_nop 7\n\t" A_SEL("%[s0]", 0) A_SEL("%[s1]", 1) A_SEL("%[s2]", 2) A_SEL("%[s3]", 3)
      : [s0] "=&v"(s0), [s1] "=&v"(s1), [s2] "=&v"(s2), [s3] "=&v"(s3), [t] "=&v"(t)
      : [kb] "v"(kb), [qb] "v"(qb)
      : VCLOB_8_31, VCLOB_32_63, SCLOB, ACLOB, "memory");
}
#define A_TR(BK, XOR) \
  "v_xor_b32 %[t], " #XOR ", %[vb]\n\t" \
  "ds_read_b64_tr_b16 v[" #BK ":" #BK "+1], %[t]\n\t" \
  "ds_read_b64_tr_b16 v[" #BK "+2:" #BK "+3], %[t] offset:4096\n\t" \
  "ds_read_b64_tr_b16 v[" #BK "+4:" #BK "+5], %[t] offset:8192\n\t" \
  "ds_read_b64_tr_b16 v[" #BK "+6:" #BK "+7], %[t] offset:12288\n\t"
#define A_PV(BK, D) \
  MF "v[" #D ":" #D "+3], v[" #BK ":" #BK "+3], %[p0], 0\n\t" \
  MF "v[" #D ":" #D "+3], v[" #BK "+4:" #BK "+7], %[p1], v[" #D ":" #D "+3]\n\t"
#define A_ST(D, O) "ds_write_b128 %[cb], v[" #D ":" #D "+3] offset:" #O "\n\t"
DEV float a_pv(v4u p0, v4u p1, int vb, int cb) {
  int t;
  float ls;
  asm volatile(
      "v_mov_b32 v60, 0x3f803f80\n\tv_mov_b32 v61, 0x3f803f80\n\tv_mov_b32 v62, 0x3f803f80\n\tv_mov_b32 v63, 0x3f803f80\n\t"
      "s_waitcnt lgkmcnt(0)\n\t"
      A_TR(16, 0) A_TR(24, 32)
      MF "v[56:59], v[60:63], %[p0], 0\n\t" MF "v[56:59], v[60:63], %[p1], v[56:59]\n\t"
      "s_waitcnt lgkmcnt(4)\n\t" A_PV(16, 32)
      A_TR(16, 64) "s_waitcnt lgkmcnt(4)\n\t" A_PV(24, 36)
      A_TR(24, 96) "s_waitcnt lgkmcnt(4)\n\t" A_PV(16, 40)
      "v_mov_b32 %[ls], v56\n\t"
      A_TR(16, 128) "s_waitcnt lgkmcnt(4)\n\t" A_PV(24, 44)
      A_TR(24, 160) "s_waitcnt lgkmcnt(4)\n\t" A_PV(16, 48)
      A_TR(16, 192) "s_waitcnt lgkmcnt(4)\n\t" A_PV(24, 52)
      A_TR(24, 224) "s_waitcnt lgkmcnt(4)\n\t" A_PV(16, 56)
      "s_waitcnt lgkmcnt(0)\n\t" A_PV(24, 60)
      "s_mov_b64 s[84:85], exec\n\ts_mov_b32 s86, 0x000f000f\n\ts_mov_b32 s87, 0x000f000f\n\ts_mov_b64 exec, s[86:87]\n\t"
      A_ST(32, 0) A_ST(36, 64) A_ST(40, 128) A_ST(44, 192) A_ST(48, 256) A_ST(52, 320) "s_nop 3\n\t" A_ST(56, 384) A_ST(60, 448)
      "s_mov_b64 exec, s[84:85]"
      : [t] "=&v"(t), [ls] "=&v"(ls)
      : [p0] "v"(p0), [p1] "v"(p1), [vb] "v"(vb), [cb] "v"(cb)
      : VCLOB_16_31, VCLOB_32_63, SCLOB, ACLOB, "memory");
  return ls;
}

DEV void dma_a(const Params& p, int b, int team, int j, int w, int lane, int q) {
  const size_t row0 = ((size_t)(b * 8 + team) * SK + 256 * j + 64 * w) * 128;
  const v4u sk = make_srd(p.kA + row0, 16384), sv = make_srd(p.vA + row0, 16384);
  const int r = lane >> 4, c = lane & 15;
  const int pk = r * 256 + ((c ^ r) << 4), pv = r * 256 + ((c ^ (2 * r)) << 4);
  unsigned keep;
  const int mk = __builtin_amdgcn_readfirstlane(L_AK + w * 16384 + q * 4096), mv = mk + (L_AV - L_AK), so = q * 4096;
  asm volatile(
      "s_mov_b32 %0, m0\n\ts_mov_b32 m0, %1\n\ts_nop 4\n\t"
      "buffer_load_dwordx4 %2, %6, %7 offen nt lds\n\t"
      "buffer_load_dwordx4 %3, %6, %7 offen offset:1024 nt lds\n\t"
      "buffer_load_dwordx4 %4, %6, %7 offen offset:2048 nt lds\n\t"
      "buffer_load_dwordx4 %5, %6, %7 offen offset:3072 nt lds\n\t"
      "s_mov_b32 m0, %0"
      : "=&s"(keep) : "s"(mk), "v"(pk), "v"(pk ^ 64), "v"(pk ^ 128), "v"(pk ^ 192), "s"(sk), "s"(so) : "memory");
  asm volatile(
      "s_mov_b32 %0, m0\n\ts_mov_b32 m0, %1\n\ts_nop 4\n\t"
      "buffer_load_dwordx4 %2, %4, %5 offen nt lds\n\t"
      "buffer_load_dwordx4 %3, %4, %5 offen offset:1024 nt lds\n\t"
      "buffer_load_dwordx4 %2, %4, %5 offen offset:2048 nt lds\n\t"
      "buffer_load_dwordx4 %3, %4, %5 offen offset:3072 nt lds\n\t"
      "s_mov_b32 m0, %0"
      : "=&s"(keep) : "s"(mv), "v"(pv), "v"(pv ^ 128), "s"(sv), "s"(so) : "memory");
}

template <int LO = 0, int HI = 8> DEV void load_w(v4u (&wv)[8], const u16* wbase, int ld, int tid) {
  const rsrc_t r = __builtin_amdgcn_make_buffer_rsrc((void*)wbase, 0, 512 * ld * 2, 0x00020000);
  const int off = (8 * (tid >> 2) * ld + 8 * (tid & 3)) * 2;
#pragma unroll
  for (int u = LO; u < HI; ++u) wv[u] = __builtin_amdgcn_raw_buffer_load_b128(r, off, u * ld * 2, 0);
}
DEV float dot2(unsigned a, unsigned b, float c) {
  typedef __bf16 bf16x2 __attribute__((ext_vector_type(2)));
  return __builtin_amdgcn_fdot2_f32_bf16(__builtin_bit_cast(bf16x2, a), __builtin_bit_cast(bf16x2, b), c, false);
}
DEV float gemv32(const v4u (&wv)[8], v4u a, LAS char* L, int tid) {
  float acc[8];
#pragma unroll
  for (int i = 0; i < 8; ++i) acc[i] = 0.f;
#pragma unroll
  for (int u = 0; u < 8; ++u) {
    const unsigned aw = a[u >> 1];
    const unsigned al = (u & 1) ? aw >> 16 : aw & 0xffffu, ah = (u & 1) ? aw & 0xffff0000u : aw << 16;
    acc[0] = dot2(wv[u].x, al, acc[0]); acc[1] = dot2(wv[u].x, ah, acc[1]);
    acc[2] = dot2(wv[u].y, al, acc[2]); acc[3] = dot2(wv[u].y, ah, acc[3]);
    acc[4] = dot2(wv[u].z, al, acc[4]); acc[5] = dot2(wv[u].z, ah, acc[5]);
    acc[6] = dot2(wv[u].w, al, acc[6]); acc[7] = dot2(wv[u].w, ah, acc[7]);
  }
#pragma unroll
  for (int i = 0; i < 8; ++i) acc[i] += dppf<0x124>(acc[i]);
#pragma unroll
  for (int i = 0; i < 8; ++i) acc[i] += dppf<0x128>(acc[i]);
  if ((tid & 15) < 4) {
    LAS f32x4* d = (LAS f32x4*)(L + L_RED + (tid >> 4) * 128 + (tid & 3) * 32);
    d[0] = f32x4{acc[0], acc[1], acc[2], acc[3]}; d[1] = f32x4{acc[4], acc[5], acc[6], acc[7]};
  }
  lds_barrier();
  float s = 0.f;
  if (tid < 64) {
    const LAS float* r = (const LAS float*)(L + L_RED) + (tid >> 5) * 256 + (tid & 31);
    s = ((r[0] + r[32]) + (r[64] + r[96])) + ((r[128] + r[160]) + (r[192] + r[224]));
    const auto sw = __builtin_amdgcn_permlane32_swap(asu(s), asu(s), false, false);
    s = asf(sw[0]) + asf(sw[1]);
  }
  return s;
}
DEV v4u ln_stage(unsigned xn, LAS char* L, int tid, int stage) {
  const float a = lo(xn), b = hi(xn);
  const float s1 = wave_sum(a + b), s2 = wave_sum(a * a + b * b);
  *(LAS unsigned*)(L + stage + tid * 4) = xn;
  if ((tid & 63) == 0) *(LAS f32x2*)(L + L_ST + (tid >> 6) * 8) = f32x2{s1, s2};
  lds_barrier();
  const f32x4 t0 = *(const LAS f32x4*)(L + L_ST), t1 = *(const LAS f32x4*)(L + L_ST + 16);
  const v4u xv = *(const LAS v4u*)(L + stage + (tid >> 2) * 16);
  const float mean = ((t0.x + t0.z) + (t1.x + t1.z)) * (1.f / DM);
  const float var = fmaxf(((t0.y + t0.w) + (t1.y + t1.w)) * (1.f / DM) - mean * mean, 0.f);
  const float mb = rbf(mean), rd = __builtin_amdgcn_rcpf(rbf(__builtin_amdgcn_sqrtf(rbf(rbf(var) + 1e-5f))));
  v4u o;
#pragma unroll
  for (int i = 0; i < 4; ++i) o[i] = f2bf(rbf(lo(xv[i]) - mb) * rd) | (f2bf(rbf(hi(xv[i]) - mb) * rd) << 16);
  return o;
}

enum { T_Q = 0, T_A = 1, T_B = 2, T_H1 = 3, T_H2 = 4 };
#define OP(T, K, DROW, NBURST, NDMA) ((unsigned long long)((T) | ((K) << 3) | (((DROW) + 1) << 5) | ((NBURST) << 7) | ((NDMA) << 10)))
constexpr int NOPS = 16, PC0 = 10;
constexpr unsigned long long prog[NOPS] = {
    OP(T_Q, 0, 0, 1, 0),
    OP(T_H2, 3, -1, 0, 0),
    OP(T_B, 1, 0, 0, 0),
    OP(T_H1, 1, 0, 0, 0),
    OP(T_H2, 1, 0, 0, 0),
    OP(T_Q, 1, 0, 0, 0),
    OP(T_A, 2, 0, 0, 1),
    OP(T_H1, 2, 0, 0, 1),
    OP(T_H2, 2, 0, 0, 1),
    OP(T_Q, 2, 0, 0, 0),
    OP(T_B, 3, 0, 1, 0),
    OP(T_Q, 3, 1, 1, 0),
    OP(T_A, 0, 1, 1, 0),
    OP(T_H1, 3, 0, 1, 0),
    OP(T_H1, 0, 1, 1, 0),
    OP(T_H2, 0, 1, 1, 0),
};
constexpr unsigned long long pack4(int i) { return prog[i] | (prog[i + 1] << 16) | (prog[i + 2] << 32) | (prog[i + 3] << 48); }

__global__ void __launch_bounds__(256, 1) __attribute__((amdgpu_num_vgpr(64))) mega_kernel(Params p) {
  extern __shared__ __attribute__((aligned(16))) char smem[];
  asm volatile("" ::: "v255", "a255");
  LAS char* L = (LAS char*)smem;
  const int w_ = __builtin_amdgcn_readfirstlane(threadIdx.x >> 6), bid_ = blockIdx.x;
  const int grp = (bid_ >> 3) & 1;
  const bool writer = (bid_ & 7) == 0;
  const rsrc_t rs = __builtin_amdgcn_make_buffer_rsrc((void*)(p.ws + (size_t)grp * W_GROUP), 0, W_GROUP * 4, 0x00020000);
#define IDS() \
  const int lane = lane_id(), w = fresh_s(w_), tid = w * 64 + lane, n16 = lane & 15, g4 = lane >> 4, bid = fresh_s(bid_), team = bid & 7, j = bid >> 4; \
  (void)tid; (void)lane; (void)n16; (void)g4; (void)w; (void)team; (void)j
#define STREAM_ARGS(R) \
  const int lk = (n16 >> 1) * 256 + (n16 & 1) * 64 + g4 * 16, lv = g4 * 2048 + n16 * 16; \
  const size_t srow = ((size_t)((grp + 2 * (R)) * 32 + 4 * team + (j >> 2)) * SK + 1024 * (j & 3) + 256 * w) * 128; \
  const v4u skB = make_srd(p.kB + srow, 65536), svB = make_srd(p.vB + srow, 65536)
#define TICK() do { \
    if (ndma != 0) { if (row < 3 && nq < 4 * (row + 1)) { IDS(); dma_a(p, grp + 2 * (row + 1), team, j, w, lane, nq & 3); ++nq; } } \
    else if (nburst != 0 && tok != 0u) { IDS(); STREAM_ARGS(strow); \
      for (int k_ = 0; k_ < nburst && nb < NHB; ++k_) { half_burst(nb, lk, lv, skB, svB); ++nb; } } } while (0)
#define SETTLE() __builtin_amdgcn_s_waitcnt(0x0f70)
#define SEND_TOK() do { if (w == 0 && row + grp < 1) { \
    const rsrc_t rso = __builtin_amdgcn_make_buffer_rsrc((void*)(p.ws + (size_t)(grp ^ 1) * W_GROUP), 0, W_GROUP * 4, 0x00020000); \
    st1(rso, (W_TOK + (team * 16 + j) * 16 + row + grp) * 4, 1u); } } while (0)
#define DMA_B2() do { if (!first && row < 3 && nq < 4 * (row + 1)) { IDS(); dma_a(p, grp + 2 * (row + 1), team, j, w, lane, nq & 3); ++nq; } } while (0)
#define CATCH_UP(N) do { while (nb < (N)) { half_burst(nb, lk, lv, skB, svB); ++nb; } } while (0)
#define TOKOFF ((W_TOK + (team * 16 + j) * 16 + strow) * 4)
#define POLL_TOK() do { if (tok == 0u && nburst != 0) tok = __builtin_amdgcn_readfirstlane(tok | ld1(rs, TOKOFF)); } while (0)

  int nb = 0;
  unsigned tok = grp == 0 ? 1u : 0u;
  {
    v4u wv[8];
    {
      IDS();
      load_w(wv, p.wqA + 512 * team + 32 * j, QD, tid);
#pragma unroll
      for (int r = 0; r < 4; ++r) *(LAS unsigned*)(L + L_U + 2048 + r * 1024 + tid * 4) = *(const unsigned*)(p.x + (grp + 2 * r) * DM + 2 * tid);
      SETTLE();
    }
    for (int r = 0; r < 4; ++r) {
      IDS();
      const unsigned xr = *(const LAS unsigned*)(L + L_U + 2048 + r * 1024 + tid * 4);
      dma_a(p, grp, team, j, w, lane, r);
      const v4u a = ln_stage(xr, L, tid, L_O);
      const float q = gemv32(wv, a, L, tid);
      if (tid < 32) st1(rs, (W_Q + (3 + r) * QD + 512 * team + 32 * j + tid) * 4, (f2bf(q) << 16) | (1u + (unsigned)r % 3u));
      if (grp == 0) { STREAM_ARGS(0); half_burst(nb, lk, lv, skB, svB); ++nb; }
    }
  }
  unsigned cap = SPIN_CAP;
  int nq = 0;

  for (int pc = 11 - NOPS; pc < 4 * NOPS + 2; ++pc) {
    const int it = (pc >> 4), idx = pc & 15;
    const unsigned long long pw = (idx & 8) ? ((idx & 4) ? pack4(12) : pack4(8)) : ((idx & 4) ? pack4(4) : pack4(0));
    const unsigned op = (unsigned)(pw >> (16 * (idx & 3))) & 0xffffu;
    const int type = op & 7, kind = (op >> 3) & 3, row = it + (int)((op >> 5) & 3) - 1, ndma = (op >> 10) & 7;
    const int strow = idx >= PC0 ? it + 1 : it;
    const int nburst = strow < 4 ? (op >> 7) & 7 : 0;
    if (row < 0 || row > 3) continue;
    tok = __builtin_amdgcn_readfirstlane(tok); nb = __builtin_amdgcn_readfirstlane(nb); nq = __builtin_amdgcn_readfirstlane(nq); cap = __builtin_amdgcn_readfirstlane(cap);
    const unsigned tag = 1u + (unsigned)row % 3u, ptag = (tag & 1u) | ((tag & 2u) << 15);
    const bool isA = (kind & 1) == 0;

    if (type == T_Q) {
      IDS();
      const bool forB = (kind & 1) == 0;
      if (!forB || w == 0) {
        const int qoff = W_Q + (kind == 3 ? 3 + row : kind) * QD + 512 * team + (forB ? 128 * (j >> 2) : 0);
        unsigned spins = 0;
        v2u qv = ld2(rs, tid * 8, qoff * 4);
        for (;;) {
          cfence();
          const v2u qn = ld2(rs, tid * 8, qoff * 4);
          if (none((((qv.x ^ tag) | (qv.y ^ tag)) & 3u) != 0u) || ++spins > cap) { if (spins > cap) cap = 0u; break; }
          POLL_TOK();
          qv = qn;
        }
        *(LAS unsigned*)(L + L_Q + tid * 4) = (qv.x >> 16) | (qv.y & 0xffff0000u);
      }
      lds_barrier();
      SETTLE(); TICK();
    } else if (type == T_A || type == T_B) {
      if (type == T_A) {
        float sc[4];
        { IDS(); a_scores(sc[0], sc[1], sc[2], sc[3], L_AK + (64 * w + n16) * 256 + ((g4 ^ n16) << 4), L_Q + (n16 & 3) * 256 + g4 * 16); }
        if (kind == 0) TICK();
        IDS();
        LAS float* comb = (LAS float*)(L + L_U) + w * 4 * CREC;
        LAS u16* pex = (LAS u16*)comb;
        const int ppos = 32 * (g4 >> 1) + 8 * (n16 >> 2) + 4 * (g4 & 1) + (n16 & 3);
        float mh[4];
#pragma unroll
        for (int h = 0; h < 4; ++h) { sc[h] = rbf(rbf(sc[h]) * SCALE) * LOG2E; mh[h] = wave_max(sc[h]); }
#pragma unroll
        for (int h = 0; h < 4; ++h) pex[h * 64 + ppos] = (u16)f2bf(ex2(sc[h] - mh[h]));
        const v4u pb0 = *(const LAS v4u*)(pex + (n16 & 3) * 64 + 8 * g4), pb1 = *(const LAS v4u*)(pex + (n16 & 3) * 64 + 32 + 8 * g4);
        const float mm = lane == 0 ? mh[0] : lane == 1 ? mh[1] : lane == 2 ? mh[2] : mh[3];
        const int q_ = (lane >> 2) & 3, p_ = lane & 3;
        const float ll = a_pv(pb0, pb1, L_AV + (64 * w + 4 * g4 + q_) * 256 + ((((p_ >> 1) ^ (2 * (4 * (g4 & 1) + q_)))) << 4) + (p_ & 1) * 8,
                              L_U + (w * 4 * CREC + n16 * CREC + 4 * g4) * 4);
        { IDS(); if (lane < 4) *(LAS f32x2*)(L + L_U + ((w * 4 + lane) * CREC + 128) * 4) = f32x2{mm, ll}; }
      } else {
        const bool first = kind == 1;
        if (first) {
          {
            IDS(); STREAM_ARGS(row);
            unsigned spins = 0;
            while (tok == 0u && ++spins < cap) { cfence(); tok = __builtin_amdgcn_readfirstlane(tok | ld1(rs, TOKOFF)); __builtin_amdgcn_s_sleep(2); }
            SEND_TOK();
            CATCH_UP(NHB);
          }
        }
        v4u qa0 = {0u, 0u, 0u, 0u}, qa1 = {0u, 0u, 0u, 0u}, msk;
        {
          IDS();
          if ((n16 & 2) == 0) {
            qa0 = *(const LAS v4u*)(L + L_Q + (4 * (n16 & 1) + g4) * 16);
            qa1 = *(const LAS v4u*)(L + L_Q + (8 + 4 * (n16 & 1) + g4) * 16);
          }
          const unsigned hm = (n16 & 1) ? 0xffff0000u : 0x0000ffffu;
          const int ri = n16 >> 1;
          msk.x = ri == 0 ? hm : 0u; msk.y = ri == 1 ? hm : 0u; msk.z = ri == 2 ? hm : 0u; msk.w = ri == 3 ? hm : 0u;
          if (first) wait_vm<63>();
        }
        BState st; st.acc = f32x4{0.f, 0.f, 0.f, 0.f}; st.m = -INFINITY; st.l = 0.f;
        b_stage_last(st, qa0, qa1, msk);
        {
          IDS(); STREAM_ARGS(row);
          const int sv_off = L_RB + w * (NSAVE * 1024) + lane * 16;
          if (first) {
            asm volatile("ds_write_b128 %0, v[236:239]\n\tds_write_b128 %0, v[240:243] offset:1024\n\tds_write_b128 %0, v[244:247] offset:2048\n\t"
                         "ds_write_b128 %0, v[248:251] offset:3072\n\tds_write_b128 %0, v[252:255] offset:4096\n\ts_waitcnt lgkmcnt(0)" ::"v"(sv_off) : "memory");
            ld_unit<6, 0, 16>(lk, lv, skB, svB, 7);
            wait_vm<63>();
          } else {
            ld_unit<6, 0, 16 - NSAVE>(lk, lv, skB, svB, 0);
            asm volatile("ds_read_b128 v[236:239], %0\n\tds_read_b128 v[240:243], %0 offset:1024\n\tds_read_b128 v[244:247], %0 offset:2048\n\t"
                         "ds_read_b128 v[248:251], %0 offset:3072\n\tds_read_b128 v[252:255], %0 offset:4096" ::"v"(sv_off) : "memory");
          }
        }
        const int nq0 = nq;
        DMA_B2(); DMA_B2();
        b_stage_012(st, qa0, qa1, msk);
        if (first) wait_vm<16>();
        DMA_B2();
        b_stage_345(st, qa0, qa1, msk);
        if (nq - nq0 == 3) wait_vm<24>(); else wait_vm<0>();
        asm volatile("s_waitcnt lgkmcnt(0)" ::: "memory");
        DMA_B2();
        b_stage_last(st, qa0, qa1, msk);
        {
          IDS();
          LAS float* comb = (LAS float*)(L + L_U) + w * 4 * CREC;
          const float lsum = wave_sum(st.l) * 0.5f;
          if (n16 < 8) {
            comb[32 * g4 + n16] = st.acc.x; comb[32 * g4 + 8 + n16] = st.acc.y; comb[32 * g4 + 16 + n16] = st.acc.z; comb[32 * g4 + 24 + n16] = st.acc.w;
          }
          if (lane == 0) *(LAS f32x2*)(comb + 128) = f32x2{st.m, lsum};
        }
        if (!first) nb = 0;
      }
      lds_barrier();
      {
        IDS();
        const bool isml = tid >= 128;
        const int h = isml ? tid - 128 : isA ? tid >> 4 : 0;
        if (isml ? h < (isA ? 4 : 1) : tid < (isA ? 64 : 32)) {
          const LAS float* cb = (const LAS float*)(L + L_U) + h * CREC;
          const f32x2 a0 = *(const LAS f32x2*)(cb + 128), a1 = *(const LAS f32x2*)(cb + 4 * CREC + 128), a2 = *(const LAS f32x2*)(cb + 8 * CREC + 128),
                      a3 = *(const LAS f32x2*)(cb + 12 * CREC + 128);
          const float M = fmaxf(fmaxf(a0.x, a1.x), fmaxf(a2.x, a3.x));
          const float f0 = ex2(a0.x - M), f1 = ex2(a1.x - M), f2 = ex2(a2.x - M), f3 = ex2(a3.x - M);
          const int rec = isA ? W_P1A + (kind >> 1) * SZ_P1A + ((team * 16 + j) * 4 + h) * RECA : W_P1B + (kind >> 1) * SZ_P1B + (team * 16 + j) * RECB;
          if (isml) {
            st2(rs, (rec + (isA ? 64 : 128)) * 4, v2u{tagw(M, tag), tagw((f0 * a0.y + f1 * a1.y) + (f2 * a2.y + f3 * a3.y), tag)});
          } else if (isA) {
            const int d = 8 * (tid & 15);
            const f32x4 o = (*(const LAS f32x4*)(cb + d) * f0 + *(const LAS f32x4*)(cb + 4 * CREC + d) * f1) +
                            (*(const LAS f32x4*)(cb + 8 * CREC + d) * f2 + *(const LAS f32x4*)(cb + 12 * CREC + d) * f3);
            const f32x4 q = (*(const LAS f32x4*)(cb + d + 4) * f0 + *(const LAS f32x4*)(cb + 4 * CREC + d + 4) * f1) +
                            (*(const LAS f32x4*)(cb + 8 * CREC + d + 4) * f2 + *(const LAS f32x4*)(cb + 12 * CREC + d + 4) * f3);
            float am = fmaxf(fmaxf(fmaxf(fabsf(o.x), fabsf(o.y)), fmaxf(fabsf(o.z), fabsf(o.w))), fmaxf(fmaxf(fabsf(q.x), fabsf(q.y)), fmaxf(fabsf(q.z), fabsf(q.w))));
            am = fmaxf(row_max(am), 1e-30f);
            const float sc = asf(tagw(am * (1.f / 16382.f), tag)), rs_ = __builtin_amdgcn_rcpf(sc);
            st4(rs, (rec + 4 * (tid & 15)) * 4, v4u{tagp(o.x * rs_, o.y * rs_, ptag), tagp(o.z * rs_, o.w * rs_, ptag), tagp(q.x * rs_, q.y * rs_, ptag), tagp(q.z * rs_, q.w * rs_, ptag)});
            if ((tid & 15) == 0) st1(rs, (rec + 66) * 4, asu(sc));
          } else {
            const int d = 4 * tid;
            const f32x4 o = (*(const LAS f32x4*)(cb + d) * f0 + *(const LAS f32x4*)(cb + 4 * CREC + d) * f1) +
                            (*(const LAS f32x4*)(cb + 8 * CREC + d) * f2 + *(const LAS f32x4*)(cb + 12 * CREC + d) * f3);
            st4(rs, (rec + d) * 4, v4u{tagw(o.x, tag), tagw(o.y, tag), tagw(o.z, tag), tagw(o.w, tag)});
          }
        }
      }
      TICK();
    } else if (type == T_H1) {
      v4u wv[8];
      unsigned pr;
      { IDS(); load_w<0, 6>(wv, (isA ? p.woA : p.woB) + (size_t)512 * team * DM + 32 * j, DM, tid); }
      {
        IDS();
        unsigned spins = 0;
        float o0 = 0.f, o1 = 0.f;
        if (isA) {
          const int base = (W_P1A + (kind >> 1) * SZ_P1A + (team * 64 + w) * RECA) * 4;
          v2u ml; unsigned o[16], sw;
          for (;;) {
            cfence();
            ml = ld2(rs, n16 * (4 * RECA * 4) + 256, base);
            sw = ld1(rs, n16 * (4 * RECA * 4) + 264, base);
#pragma unroll
            for (int s = 0; s < 16; ++s) o[s] = ld1(rs, lane * 4, base + s * (4 * RECA * 4));
            POLL_TOK();
            unsigned aa = o[0], oo = o[0];
#pragma unroll
            for (int s = 1; s < 16; ++s) { aa &= o[s]; oo |= o[s]; }
            const unsigned bad = (((aa ^ ptag) | (oo ^ ptag)) & 0x10001u) | (((ml.x ^ tag) | (ml.y ^ tag) | (sw ^ tag)) & 3u);
            if (none(bad != 0u) || ++spins > cap) { if (spins > cap) cap = 0u; break; }
          }
          cfence();
          load_w<6, 8>(wv, p.woA + (size_t)512 * team * DM + 32 * j, DM, tid);
          const float m = asf(ml.x), M = row_max(m), wt = ex2(m - M);
          const float f = wt * asf(sw) * __builtin_amdgcn_rcpf(row_sum(wt * asf(ml.y)));
#pragma unroll
          for (int s = 0; s < 16; ++s) {
            const float fs = rdl(f, s);
            o0 = fmaf(fs, (float)((int)(o[s] << 16) >> 17), o0); o1 = fmaf(fs, (float)((int)o[s] >> 17), o1);
          }
        } else {
          const int base = (W_P1B + (kind >> 1) * SZ_P1B + (team * 16 + 4 * w) * RECB) * 4;
          load_w<6, 8>(wv, p.woB + (size_t)512 * team * DM + 32 * j, DM, tid);
          v2u ml, o[4];
          for (;;) {
            cfence();
            ml = ld2(rs, (lane & 3) * (RECB * 4) + 512, base);
#pragma unroll
            for (int s = 0; s < 4; ++s) o[s] = ld2(rs, lane * 8, base + s * (RECB * 4));
            POLL_TOK();
            unsigned aa = ml.x & ml.y, oo = ml.x | ml.y;
#pragma unroll
            for (int s = 0; s < 4; ++s) { aa &= o[s].x & o[s].y; oo |= o[s].x | o[s].y; }
            if (none((((aa ^ tag) | (oo ^ tag)) & 3u) != 0u) || ++spins > cap) { if (spins > cap) cap = 0u; break; }
          }
          float m = asf(ml.x), M = fmaxf(m, dppf<0xB1>(m)); M = fmaxf(M, dppf<0x4E>(M));
          const float wt = ex2(m - M);
          float ls = wt * asf(ml.y); ls += dppf<0xB1>(ls); ls += dppf<0x4E>(ls);
          const float f = wt * __builtin_amdgcn_rcpf(ls);
#pragma unroll
          for (int s = 0; s < 4; ++s) { const float fs = rdl(f, s); o0 = fmaf(fs, asf(o[s].x), o0); o1 = fmaf(fs, asf(o[s].y), o1); }
        }
        pr = f2bf(o0) | (f2bf(o1) << 16);
      }
      { IDS(); *(LAS unsigned*)(L + L_O + tid * 4) = pr; }
      SETTLE(); TICK();
      IDS();
      lds_barrier();
      const float y = gemv32(wv, *(const LAS v4u*)(L + L_O + (tid >> 2) * 16), L, tid);
      if (tid < 32) st1(rs, (W_Y + (kind * 8 + team) * DM + 32 * j + tid) * 4, tagw(y, tag));
      TICK();
    } else if (kind == 3) {
      if (writer && w_ == 0) {
        IDS();
        unsigned yv[8], spins = 0;
        for (;;) {
          cfence();
#pragma unroll
          for (int k = 0; k < 8; ++k) yv[k] = ld1(rs, (lane & 31) * 4, (W_Y + (3 * 8 + k) * DM + 32 * j) * 4);
          unsigned aa = yv[0], oo = yv[0];
#pragma unroll
          for (int k = 1; k < 8; ++k) { aa &= yv[k]; oo |= yv[k]; }
          if (none((((aa ^ tag) | (oo ^ tag)) & 3u) != 0u) || ++spins > cap) { if (spins > cap) cap = 0u; break; }
        }
        const float ys = ((asf(yv[0]) + asf(yv[1])) + (asf(yv[2]) + asf(yv[3]))) + ((asf(yv[4]) + asf(yv[5])) + (asf(yv[6]) + asf(yv[7])));
        if (lane < 32) p.out[(grp + 2 * row) * DM + 32 * j + lane] = (u16)f2bf(bf2f(*(const LAS u16*)(L + L_XS + lane * 2)) + rbf(ys));
      }
    } else {
      v4u wv[8];
      unsigned xo;
      {
        IDS();
        load_w(wv, (isA ? p.wqB : p.wqA) + 512 * team + 32 * j, QD, tid);
        xo = kind == 0 ? *(const unsigned*)(p.x + (grp + 2 * row) * DM + 2 * tid) : *(const LAS unsigned*)(L + L_XP + tid * 4);
      }
      IDS();
      v2u yv[8];
      unsigned spins = 0;
      for (;;) {
        cfence();
#pragma unroll
        for (int k = 0; k < 8; ++k) yv[k] = ld2(rs, tid * 8, (W_Y + (kind * 8 + k) * DM) * 4);
        POLL_TOK();
        unsigned aa = ~0u, oo = 0u;
#pragma unroll
        for (int k = 0; k < 8; ++k) { aa &= yv[k].x & yv[k].y; oo |= yv[k].x | yv[k].y; }
        if (none((((aa ^ tag) | (oo ^ tag)) & 3u) != 0u) || ++spins > cap) { if (spins > cap) cap = 0u; break; }
      }
      const float y0 = ((asf(yv[0].x) + asf(yv[1].x)) + (asf(yv[2].x) + asf(yv[3].x))) + ((asf(yv[4].x) + asf(yv[5].x)) + (asf(yv[6].x) + asf(yv[7].x)));
      const float y1 = ((asf(yv[0].y) + asf(yv[1].y)) + (asf(yv[2].y) + asf(yv[3].y))) + ((asf(yv[4].y) + asf(yv[5].y)) + (asf(yv[6].y) + asf(yv[7].y)));
      const unsigned xn = f2bf(lo(xo) + rbf(y0)) | (f2bf(hi(xo) + rbf(y1)) << 16);
      if (kind == 2 && (tid >> 4) == j) *(LAS unsigned*)(L + L_XS + (tid & 15) * 4) = xn;
      SETTLE(); TICK();
      const v4u a = ln_stage(xn, L, tid, L_XP);
      const float q = gemv32(wv, a, L, tid);
      if (tid < 32) st1(rs, (W_Q + kind * QD + 512 * team + 32 * j + tid) * 4, (f2bf(q) << 16) | tag);
      TICK();
    }
  }
}
}

extern "C" void kernel_launch(void* const* d_in, const int* in_sizes, int n_in, void* d_out, int out_size, void* d_ws, size_t ws_size, hipStream_t stream) {
  typedef const mega::u16* cp;
  mega::Params p;
  p.x = (cp)d_in[0]; p.kA = (cp)d_in[1]; p.vA = (cp)d_in[2]; p.wqA = (cp)d_in[3]; p.woA = (cp)d_in[4];
  p.kB = (cp)d_in[6]; p.vB = (cp)d_in[7]; p.wqB = (cp)d_in[8]; p.woB = (cp)d_in[9];
  p.out = (mega::u16*)d_out; p.ws = (unsigned*)d_ws;
  (void)hipMemsetAsync(d_ws, 0, mega::WS_BYTES, stream);
  (void)hipFuncSetAttribute((const void*)mega::mega_kernel, hipFuncAttributeMaxDynamicSharedMemorySize, 163840);
  mega::mega_kernel<<<256, 256, 163840, stream>>>(p);
}
```
